```python
import torch

def _softmax(x, axis=-1):
    return torch.softmax(x, dim=axis)
def _gelu(x):
    return torch.nn.functional.gelu(x, approximate="tanh")
def _silu(x):
    return torch.nn.functional.silu(x)
def _sigmoid(x):
    return torch.sigmoid(x)
def _layernorm(x, axis=-1, eps=1e-5):
    m = x.mean(dim=axis, keepdim=True)
    v = x.var(dim=axis, keepdim=True, unbiased=False)
    return (x - m) / torch.sqrt(v + eps)
def _scatter_add(tgt, idx, src, axis=0):
    return tgt.clone().index_add_(axis, idx, src)
def _segment_sum(src, seg, n):
    out = torch.zeros((n,) + src.shape[1:], dtype=src.dtype)
    return out.index_add_(0, seg, src)
def _pad(x, pw):
    flat = tuple(v for pair in reversed(pw) for v in pair)
    return torch.nn.functional.pad(x, flat)

kernel_name = 'concat_heads__mha_block_cached__gqa_block_cached__near_ridge'

def setup_inputs():

    _w = (lambda a: (lambda t: t.to(torch.bfloat16) if t.is_floating_point() else t)(torch.as_tensor(a)))
    def _A_setup():
        B, Sq, D = 16, 32, 1024
        H, Sk, Dh = 32, 32, 128
        return {
            "x": _w((lambda *s: torch.randn(*s, dtype=torch.bfloat16) * 0.8)(B, Sq, D)),
            "k_cache": _w((lambda *s: torch.randn(*s, dtype=torch.bfloat16) * 0.8)(B, H, Sk, Dh)),
            "v_cache": _w((lambda *s: torch.randn(*s, dtype=torch.bfloat16) * 0.05)(B, H, Sk, Dh)),
            "wq": _w((lambda *s: torch.randn(*s, dtype=torch.bfloat16) * 0.8)(D, H * Dh) * (D ** -0.5)),
            "wo": _w((lambda *s: torch.randn(*s, dtype=torch.bfloat16) * 0.05)(H * Dh, D) * ((H * Dh) ** -0.5)),
        }
    _A_in = _A_setup()

    def _B_setup():
        B, Sq, D = 16, 32, 1024
        Hq, Hkv, Sk, Dh = 32, 32, 32, 128
        assert Hq % Hkv == 0, (Hq, Hkv)
        return {
            "x": _w((lambda *s: torch.randn(*s, dtype=torch.bfloat16) * 0.8)(B, Sq, D)),
            "k_cache": _w((lambda *s: torch.randn(*s, dtype=torch.bfloat16) * 0.8)(B, Hkv, Sk, Dh)),
            "v_cache": _w((lambda *s: torch.randn(*s, dtype=torch.bfloat16) * 0.05)(B, Hkv, Sk, Dh)),
            "wq": _w((lambda *s: torch.randn(*s, dtype=torch.bfloat16) * 0.8)(D, Hq * Dh) * (D ** -0.5)),
            "wo": _w((lambda *s: torch.randn(*s, dtype=torch.bfloat16) * 0.05)(Hq * Dh, D) * ((Hq * Dh) ** -0.5)),
        }
    _B_in = _B_setup()

    return {
        "A_x": _A_in["x"],
        "A_k_cache": _A_in["k_cache"],
        "A_v_cache": _A_in["v_cache"],
        "A_wq": _A_in["wq"],
        "A_wo": _A_in["wo"],
        "B_x": _B_in["x"],
        "B_k_cache": _B_in["k_cache"],
        "B_v_cache": _B_in["v_cache"],
        "B_wq": _B_in["wq"],
        "B_wo": _B_in["wo"],
    }

def reference(A_x, A_k_cache, A_v_cache, A_wq, A_wo, B_x, B_k_cache, B_v_cache, B_wq, B_wo):
    def _A_ref(x, k_cache, v_cache, wq, wo):
        B, Sq, D = x.shape
        H, Dh = 32, 128
        q = (lambda x, *d: x.permute(*d))((x @ wq).reshape(B, Sq, H, Dh), 0, 2, 1, 3)
        scores = torch.einsum("bhqd,bhkd->bhqk", q, k_cache) * (Dh ** -0.5)
        p = _softmax(scores, axis=-1)
        o = torch.einsum("bhqk,bhkd->bhqd", p, v_cache)
        return (lambda x, *d: x.permute(*d))(o, 0, 2, 1, 3).reshape(B, Sq, H * Dh) @ wo

    def _B_ref(x, k_cache, v_cache, wq, wo):
        B, Sq, D = x.shape
        Hq, Dh = 32, 128
        Hkv = k_cache.shape[1]
        rep = Hq // Hkv
        q = (lambda x, *d: x.permute(*d))((x @ wq).reshape(B, Sq, Hq, Dh), 0, 2, 1, 3)
        k = (lambda x, n, axis: torch.repeat_interleave(x, n, dim=axis))(k_cache, rep, axis=1)
        v = (lambda x, n, axis: torch.repeat_interleave(x, n, dim=axis))(v_cache, rep, axis=1)
        scores = torch.einsum("bhqd,bhkd->bhqk", q, k) * (Dh ** -0.5)
        p = _softmax(scores, axis=-1)
        o = torch.einsum("bhqk,bhkd->bhqd", p, v)
        return (lambda x, *d: x.permute(*d))(o, 0, 2, 1, 3).reshape(B, Sq, Hq * Dh) @ wo

    a = _A_ref(A_x, A_k_cache, A_v_cache, A_wq, A_wo)
    b = _B_ref(B_x, B_k_cache, B_v_cache, B_wq, B_wo)
    return torch.concatenate([a, b], axis=-1)
```

```cpp
#include <hip/hip_runtime.h>
#include <hip/hip_bf16.h>
typedef __hip_bfloat16 bf16;
__device__ inline float b2f(bf16 x){return __bfloat162float(x);}
__device__ inline float rbf(float x){return __bfloat162float(__float2bfloat16(x));}

__global__ void k_q(const bf16* x,const bf16* w,bf16* q,int M,int N,int K){
  int n=blockIdx.x*256+threadIdx.x; int m=blockIdx.y;
  float acc=0; for(int k=0;k<K;k++) acc+=b2f(x[m*K+k])*b2f(w[(long)k*N+n]);
  q[(long)m*N+n]=__float2bfloat16(acc);
}
__global__ void k_attn(const bf16* q,const bf16* kc,const bf16* vc,bf16* o){
  int bh=blockIdx.x; int b=bh/32,h=bh%32;
  __shared__ float p[32][33];
  int t=threadIdx.x;
  int qi=t/32,ki=t%32;
  float s=0; for(int d=0;d<128;d++) s+=b2f(q[(long)(b*32+qi)*4096+h*128+d])*b2f(kc[((long)(b*32+h)*32+ki)*128+d]);
  s=rbf(s); s=rbf(s*0.08838834764831845f);
  p[qi][ki]=s; __syncthreads();
  float m=-1e30f; for(int j=0;j<32;j++) m=fmaxf(m,p[qi][j]);
  float sum=0; for(int j=0;j<32;j++) sum+=__expf(p[qi][j]-m);
  float pr=rbf(__expf(s-m)/sum);
  __syncthreads(); p[qi][ki]=pr; __syncthreads();
  for(int d=ki;d<128;d+=32){ float a=0; for(int j=0;j<32;j++) a+=p[qi][j]*b2f(vc[((long)(b*32+h)*32+j)*128+d]);
    o[(long)(b*32+qi)*4096+h*128+d]=__float2bfloat16(a);}
}
__global__ void k_o(const bf16* o,const bf16* w,bf16* out,int off){
  int n=blockIdx.x*256+threadIdx.x; int m=blockIdx.y;
  float acc=0; for(int k=0;k<4096;k++) acc+=b2f(o[(long)m*4096+k])*b2f(w[(long)k*1024+n]);
  out[(long)m*2048+off+n]=__float2bfloat16(acc);
}
extern "C" void kernel_launch(void* const* d_in, const int* in_sizes, int n_in,
                              void* d_out, int out_size, void* d_ws, size_t ws_size,
                              hipStream_t stream) {
  bf16* q=(bf16*)d_ws; bf16* o=q+512*4096;
  for(int br=0;br<2;br++){
    const bf16* x=(const bf16*)d_in[br*5+0]; const bf16* kc=(const bf16*)d_in[br*5+1]; const bf16* vc=(const bf16*)d_in[br*5+2];
    const bf16* wq=(const bf16*)d_in[br*5+3]; const bf16* wo=(const bf16*)d_in[br*5+4];
    k_q<<<dim3(16,512),256,0,stream>>>(x,wq,q,512,4096,1024);
    k_attn<<<512,1024,0,stream>>>(q,kc,vc,o);
    k_o<<<dim3(4,512),256,0,stream>>>(o,wo,(bf16*)d_out,br*1024);
  }
}
```

```cpp
#include <hip/hip_runtime.h>
#include <hip/hip_bf16.h>
typedef __bf16 bf16x8 __attribute__((ext_vector_type(8)));
typedef __bf16 bf16x4 __attribute__((ext_vector_type(4)));
typedef __bf16 bf16x2 __attribute__((ext_vector_type(2)));
typedef float f32x16 __attribute__((ext_vector_type(16)));
typedef float f32x2 __attribute__((ext_vector_type(2)));
typedef unsigned u32x4 __attribute__((ext_vector_type(4)));
#define LAS __attribute__((address_space(3)))
#define GAS __attribute__((address_space(1)))
typedef GAS unsigned long long gu64;
#define DI __device__ __forceinline__
#define RLX_AGENT __ATOMIC_RELAXED, __HIP_MEMORY_SCOPE_AGENT
constexpr int SLOT=32768, NSLOT=5, LDS_BYTES=SLOT*NSLOT;
constexpr size_t WS_P=0, WS_PART=2u<<20, WS_FLAG=8u<<20, WS_EPOCH=WS_FLAG+(1u<<20);

struct Args { const void* x[2]; const void* k[2]; const void* v[2]; const void* wq[2]; const void* wo[2]; void* out; char* ws; };

DI unsigned cvtpk(float lo,float hi){ f32x2 v={lo,hi}; bf16x2 b=__builtin_convertvector(v,bf16x2); return __builtin_bit_cast(unsigned,b); }
DI bf16x8 pack8(const f32x16& a,int s){ u32x4 w; w[0]=cvtpk(a[8*s],a[8*s+1]); w[1]=cvtpk(a[8*s+2],a[8*s+3]); w[2]=cvtpk(a[8*s+4],a[8*s+5]); w[3]=cvtpk(a[8*s+6],a[8*s+7]); return __builtin_bit_cast(bf16x8,w); }
DI bf16x8 lds128(unsigned a){ return *(const LAS bf16x8*)(size_t)a; }
DI bf16x8 ldstr(unsigned a){ bf16x4 lo=__builtin_amdgcn_ds_read_tr16_b64_v4bf16((LAS bf16x4*)(size_t)a); bf16x4 hi=__builtin_amdgcn_ds_read_tr16_b64_v4bf16((LAS bf16x4*)(size_t)(a+512)); return __builtin_shufflevector(lo,hi,0,1,2,3,4,5,6,7); }
#define MFMA(a,b,c) __builtin_amdgcn_mfma_f32_32x32x16_bf16((a),(b),(c),0,0,0)

DI void dma4(unsigned m0v, const unsigned (&vo)[4], __amdgpu_buffer_rsrc_t rs, unsigned so){
  asm volatile("s_mov_b32 m0, %0\n\ts_nop 0\n\t"
    "buffer_load_dwordx4 %1, %5, %6 offen lds\n\t"
    "buffer_load_dwordx4 %2, %5, %6 offen offset:1024 lds\n\t"
    "buffer_load_dwordx4 %3, %5, %6 offen offset:2048 lds\n\t"
    "buffer_load_dwordx4 %4, %5, %6 offen offset:3072 lds"
    :: "s"(m0v),"v"(vo[0]),"v"(vo[1]),"v"(vo[2]),"v"(vo[3]),"s"(rs),"s"(so) : "memory");
}
#define WAITV(n) asm volatile("s_waitcnt vmcnt(" #n ")":::"memory")
#define BAR() do{ asm volatile("s_waitcnt lgkmcnt(0)":::"memory"); __builtin_amdgcn_s_barrier(); asm volatile("":::"memory"); }while(0)

struct LaneC { unsigned aR[4], aT[2]; };
DI LaneC lane_consts(unsigned ldsb,int l,int w){
  LaneC c; const int h=l>>5, bp=w>>1, ch=w&1, r=l&31;
  for(int ks=0;ks<4;ks++) c.aR[ks]=ldsb+bp*8192+r*128+16*((2*ks+h)^((r>>1)&7));
  const int blk=(l>>4)&1,q=(l&15)>>2,p=l&3, sp=2*(p&1)+(p>>1);
  for(int ct=0;ct<2;ct++) c.aT[ct]=ldsb+16384+ch*8192+(8*h+q)*128+16*((4*ct+2*blk+(sp>>1))^(4*((q>>1)&1)))+8*(sp&1);
  return c;
}

template<int PHASE> DI void stage_mma(const LaneC& c, unsigned so, f32x16 (&acc)[2][2]){
  #pragma unroll
  for(int ks=0;ks<4;ks++){
    bf16x8 R0=lds128(c.aR[ks]+so), R1=lds128(c.aR[ks]+so+4096);
    bf16x8 T0=ldstr(c.aT[0]+so+ks*2048), T1=ldstr(c.aT[1]+so+ks*2048);
    if(PHASE==1){
      acc[0][0]=MFMA(T0,R0,acc[0][0]); acc[0][1]=MFMA(T0,R1,acc[0][1]); acc[1][0]=MFMA(T1,R0,acc[1][0]); acc[1][1]=MFMA(T1,R1,acc[1][1]);
    } else {
      acc[0][0]=MFMA(R0,T0,acc[0][0]); acc[0][1]=MFMA(R0,T1,acc[0][1]); acc[1][0]=MFMA(R1,T0,acc[1][0]); acc[1][1]=MFMA(R1,T1,acc[1][1]);
    }
  }
}

__global__ __launch_bounds__(256,1) void k1(Args a){
  extern __shared__ __attribute__((aligned(16))) char smem[];
  const int tid=threadIdx.x,l=tid&63; const int w=__builtin_amdgcn_readfirstlane(tid>>6);
  const int bid=blockIdx.x,g=bid&7,idx=bid>>3,br=g>>2,ksp=g&3,r=idx&3,hl=idx>>2,head=8*ksp+hl;
  const unsigned ldsb=(unsigned)(size_t)(LAS char*)smem;
  const LaneC c=lane_consts(ldsb,l,w);
  const __amdgpu_buffer_rsrc_t rx=__builtin_amdgcn_make_buffer_rsrc((void*)a.x[br],0,512*1024*2,0x00020000);
  const __amdgpu_buffer_rsrc_t rw=__builtin_amdgcn_make_buffer_rsrc((void*)a.wq[br],0,1024*4096*2,0x00020000);
  const __amdgpu_buffer_rsrc_t rk=__builtin_amdgcn_make_buffer_rsrc((void*)a.k[br],0,16*32*32*128*2,0x00020000);
  unsigned voR[4],voT[4];
  #pragma unroll
  for(int i=0;i<4;i++){
    int row=32*w+8*i+(l>>3); voR[i]=(r*128+row)*2048+16*((l&7)^((4*i+(l>>4))&7))-1024*i;
    int rg=4*(w&1)+i, krow=8*rg+(l>>3); voT[i]=krow*8192+head*256+(w>>1)*128+16*((l&7)^(4*((l>>4)&1)))-1024*i;
  }
  const int bp=w>>1,ch=w&1,h=l>>5;
  bf16x8 kf[2][2][2];
  #pragma unroll
  for(int b=0;b<2;b++) for(int ct=0;ct<2;ct++) for(int s=0;s<2;s++){
    int batch=4*r+2*bp+b; unsigned off=((batch*32+head)*32+(l&31))*256+ch*128+ct*64+s*32+16*h;
    kf[b][ct][s]=__builtin_bit_cast(bf16x8,__builtin_amdgcn_raw_buffer_load_b128(rk,off,0,0)); }
  asm volatile("":: "v"(kf[0][0][0]),"v"(kf[0][0][1]),"v"(kf[0][1][0]),"v"(kf[0][1][1]),"v"(kf[1][0][0]),"v"(kf[1][0][1]),"v"(kf[1][1][0]),"v"(kf[1][1][1]));
  auto issue=[&](int u){ unsigned sb=ldsb+(u%NSLOT)*SLOT+w*4096; dma4(sb,voR,rx,u*128); dma4(sb+16384,voT,rw,u*524288); };
  issue(0);issue(1);issue(2);issue(3);
  f32x16 acc[2][2]={};
  #pragma unroll
  for(int u=0;u<16;u++){
    if(u<=12) WAITV(24); else if(u==13) WAITV(16); else if(u==14) WAITV(8); else WAITV(0);
    BAR();
    if(u+4<16) issue(u+4);
    stage_mma<1>(c,(u%NSLOT)*SLOT,acc);
  }
  f32x16 sp[2]={};
  #pragma unroll
  for(int b=0;b<2;b++) for(int ct=0;ct<2;ct++) for(int s=0;s<2;s++) sp[b]=MFMA(kf[b][ct][s],pack8(acc[ct][b],s),sp[b]);
  BAR();
  { const f32x16& o= ch? sp[0]:sp[1];
    #pragma unroll
    for(int i=0;i<4;i++){ u32x4 v={__float_as_uint(o[4*i]),__float_as_uint(o[4*i+1]),__float_as_uint(o[4*i+2]),__float_as_uint(o[4*i+3])}; *(LAS u32x4*)(size_t)(ldsb+w*4096+i*1024+l*16)=v; } }
  BAR();
  f32x16 S= ch? sp[1]:sp[0];
  #pragma unroll
  for(int i=0;i<4;i++){ u32x4 v=*(const LAS u32x4*)(size_t)(ldsb+(w^1)*4096+i*1024+l*16); for(int e=0;e<4;e++) S[4*i+e]+=__uint_as_float(v[e]); }
  float m=S[0];
  #pragma unroll
  for(int i=1;i<16;i++) m=fmaxf(m,S[i]);
  m=fmaxf(m,__shfl_xor(m,32));
  const float sc=0.08838834764831845f*1.4426950408889634f; float sum=0;
  #pragma unroll
  for(int i=0;i<16;i++){ S[i]=__builtin_amdgcn_exp2f((S[i]-m)*sc); sum+=S[i]; }
  sum+=__shfl_xor(sum,32);
  float inv=1.0f/sum;
  #pragma unroll
  for(int i=0;i<16;i++) S[i]*=inv;
  const int batch=4*r+2*bp+ch;
  char* pp=a.ws+WS_P+(size_t)((br*16+batch)*32+head)*2048+l*16;
  *(bf16x8*)pp=pack8(S,0); *(bf16x8*)(pp+1024)=pack8(S,1);
}

__global__ __launch_bounds__(256,1) void k2(Args a){
  extern __shared__ __attribute__((aligned(16))) char smem[];
  const int tid=threadIdx.x,l=tid&63; const int w=__builtin_amdgcn_readfirstlane(tid>>6);
  const int bid=blockIdx.x,g=bid&7,idx=bid>>3,br=g>>2,ksp=g&3,r=idx&3,j=idx>>2;
  const unsigned ldsb=(unsigned)(size_t)(LAS char*)smem;
  const LaneC c=lane_consts(ldsb,l,w);
  const __amdgpu_buffer_rsrc_t rv=__builtin_amdgcn_make_buffer_rsrc((void*)a.v[br],0,16*32*32*128*2,0x00020000);
  const __amdgpu_buffer_rsrc_t rw=__builtin_amdgcn_make_buffer_rsrc((void*)a.wo[br],0,4096*1024*2,0x00020000);
  const __amdgpu_buffer_rsrc_t rws=__builtin_amdgcn_make_buffer_rsrc((void*)a.ws,0,16u<<20,0x00020000);
  gu64* ep=(gu64*)(a.ws+WS_EPOCH+((br*4+r)*8+j)*256);
  const unsigned long long tag=__hip_atomic_load(ep,RLX_AGENT)+1ull;
  unsigned voR[4],voT[4];
  #pragma unroll
  for(int i=0;i<4;i++){
    int key=8*i+(l>>3); voR[i]=((4*r+w)*32*32+key)*256+16*((l&7)^((4*i+(l>>4))&7))-1024*i;
    int rg=4*(w&1)+i, krow=8*rg+(l>>3); voT[i]=krow*2048+j*256+(w>>1)*128+16*((l&7)^(4*((l>>4)&1)))-1024*i;
  }
  const int bp=w>>1,ch=w&1,h=l>>5;
  bf16x8 pf[2][8][2];
  #pragma unroll
  for(int b=0;b<2;b++) for(int hh=0;hh<8;hh++) for(int s=0;s<2;s++){
    unsigned off=WS_P+((br*16+4*r+2*bp+b)*32+8*ksp+hh)*2048+s*1024+l*16;
    pf[b][hh][s]=__builtin_bit_cast(bf16x8,__builtin_amdgcn_raw_buffer_load_b128(rws,off,0,0)); }
  #pragma unroll
  for(int b=0;b<2;b++) for(int hh=0;hh<8;hh+=4) asm volatile("":: "v"(pf[b][hh][0]),"v"(pf[b][hh][1]),"v"(pf[b][hh+1][0]),"v"(pf[b][hh+1][1]),"v"(pf[b][hh+2][0]),"v"(pf[b][hh+2][1]),"v"(pf[b][hh+3][0]),"v"(pf[b][hh+3][1]));
  auto issue=[&](int u){ unsigned sb=ldsb+(u%NSLOT)*SLOT+w*4096; dma4(sb,voR,rv,(8*ksp+(u>>1))*8192+(u&1)*128); dma4(sb+16384,voT,rw,(ksp*1024+u*64)*2048); };
  issue(0);issue(1);issue(2);issue(3);
  f32x16 o[2][2]={};
  f32x16 acc[2][2];
  #pragma unroll
  for(int u=0;u<16;u++){
    if(u<=12) WAITV(24); else if(u==13) WAITV(16); else if(u==14) WAITV(8); else WAITV(0);
    BAR();
    if(u+4<16) issue(u+4);
    if((u&1)==0){ for(int b=0;b<2;b++) for(int ct=0;ct<2;ct++) acc[b][ct]=f32x16{}; }
    stage_mma<2>(c,(u%NSLOT)*SLOT,acc);
    if(u&1){
      #pragma unroll
      for(int b=0;b<2;b++) for(int ct=0;ct<2;ct++) for(int s=0;s<2;s++) o[b][ct]=MFMA(pack8(acc[b][ct],s),pf[b][u>>1][s],o[b][ct]);
    }
  }
  auto box=[&](int src_s,int dst_s)->unsigned{ int wg=((idx<<3)|(br*4+src_s)); return (unsigned)WS_PART+(((wg*4+w)*3)+((dst_s-src_s-1)&3))*2048; };
  #pragma unroll
  for(int u=0;u<4;u++){ if(u!=ksp){
      const f32x16& t=o[u>>1][u&1];
      __builtin_amdgcn_raw_buffer_store_b128(__builtin_bit_cast(u32x4,pack8(t,0)),rws,box(ksp,u)+l*16,0,16);
      __builtin_amdgcn_raw_buffer_store_b128(__builtin_bit_cast(u32x4,pack8(t,1)),rws,box(ksp,u)+1024+l*16,0,16); } }
  WAITV(0);
  gu64* flags=(gu64*)(a.ws+WS_FLAG);
  if(l==0) __hip_atomic_store(flags+(bid*4+w)*16,tag,RLX_AGENT);
  { int ss=(ksp+1+(l%3))&3; gu64* f=flags+((((idx<<3)|(br*4+ss))*4+w)*16);
    for(int spin=0;spin<(1<<22);spin++){ unsigned long long v=__hip_atomic_load(f,RLX_AGENT); if(__all(v==tag)) break; __builtin_amdgcn_s_sleep(1);} }
  asm volatile("":::"memory");
  f32x16 fin= ksp==0? o[0][0] : ksp==1? o[0][1] : ksp==2? o[1][0] : o[1][1];
  #pragma unroll
  for(int d=1;d<4;d++){ int ss=(ksp+d)&3;
    #pragma unroll
    for(int s=0;s<2;s++){ u32x4 v=__builtin_amdgcn_raw_buffer_load_b128(rws,box(ss,ksp)+s*1024+l*16,0,16);
      #pragma unroll
      for(int e=0;e<4;e++){ fin[8*s+2*e]+=__uint_as_float(v[e]<<16); fin[8*s+2*e+1]+=__uint_as_float(v[e]&0xffff0000u); } } }
  { const int batch=4*r+2*bp+(ksp>>1); char* op=(char*)a.out+(size_t)(batch*32+(l&31))*4096+br*2048+(j*128+ch*64+(ksp&1)*32+8*h)*2;
    *(bf16x8*)op=pack8(fin,0); *(bf16x8*)(op+32)=pack8(fin,1); }
  if(ksp==0&&tid==0) __hip_atomic_store(ep,tag,RLX_AGENT);
}

extern "C" void kernel_launch(void* const* d_in, const int* in_sizes, int n_in, void* d_out, int out_size, void* d_ws, size_t ws_size, hipStream_t stream){
  Args a;
  for(int b=0;b<2;b++){ a.x[b]=d_in[5*b]; a.k[b]=d_in[5*b+1]; a.v[b]=d_in[5*b+2]; a.wq[b]=d_in[5*b+3]; a.wo[b]=d_in[5*b+4]; }
  a.out=d_out; a.ws=(char*)d_ws;
  hipFuncSetAttribute((const void*)k1,hipFuncAttributeMaxDynamicSharedMemorySize,LDS_BYTES);
  hipFuncSetAttribute((const void*)k2,hipFuncAttributeMaxDynamicSharedMemorySize,LDS_BYTES);
  k1<<<256,256,LDS_BYTES,stream>>>(a);
  k2<<<256,256,LDS_BYTES,stream>>>(a);
}
```

```cpp
#define POL_X 0
#define POL_WQ 0
#define POL_K 0
#define POL_V 0
#define POL_WO 0
#include <hip/hip_runtime.h>
#include <hip/hip_bf16.h>
typedef __bf16 bf16x8 __attribute__((ext_vector_type(8)));
typedef __bf16 bf16x4 __attribute__((ext_vector_type(4)));
typedef __bf16 bf16x2 __attribute__((ext_vector_type(2)));
typedef float f32x16 __attribute__((ext_vector_type(16)));
typedef float f32x2 __attribute__((ext_vector_type(2)));
typedef unsigned u32x4 __attribute__((ext_vector_type(4)));
#define LAS __attribute__((address_space(3)))
#define GAS __attribute__((address_space(1)))
typedef GAS unsigned long long gu64;
#define DI __device__ __forceinline__
#define RLX_AGENT __ATOMIC_RELAXED, __HIP_MEMORY_SCOPE_AGENT
constexpr int SLOT=32768, NSLOT=5, LDS_BYTES=SLOT*NSLOT;
constexpr size_t WS_P=0, WS_PART=2u<<20, WS_FLAG=8u<<20, WS_EPOCH=WS_FLAG+(1u<<20);

struct Args { const void* x[2]; const void* k[2]; const void* v[2]; const void* wq[2]; const void* wo[2]; void* out; char* ws; };

DI unsigned cvtpk(float lo,float hi){ f32x2 v={lo,hi}; bf16x2 b=__builtin_convertvector(v,bf16x2); return __builtin_bit_cast(unsigned,b); }
DI bf16x8 pack8(const f32x16& a,int s){ u32x4 w; w[0]=cvtpk(a[8*s],a[8*s+1]); w[1]=cvtpk(a[8*s+2],a[8*s+3]); w[2]=cvtpk(a[8*s+4],a[8*s+5]); w[3]=cvtpk(a[8*s+6],a[8*s+7]); return __builtin_bit_cast(bf16x8,w); }
DI bf16x8 lds128(unsigned a){ return *(const LAS bf16x8*)(size_t)a; }
DI bf16x8 ldstr(unsigned a){ bf16x4 lo=__builtin_amdgcn_ds_read_tr16_b64_v4bf16((LAS bf16x4*)(size_t)a); bf16x4 hi=__builtin_amdgcn_ds_read_tr16_b64_v4bf16((LAS bf16x4*)(size_t)(a+512)); return __builtin_shufflevector(lo,hi,0,1,2,3,4,5,6,7); }
#define MFMA(a,b,c) __builtin_amdgcn_mfma_f32_32x32x16_bf16((a),(b),(c),0,0,0)

template<int NT> DI void dma4(unsigned m0v, const unsigned (&vo)[4], __amdgpu_buffer_rsrc_t rs, unsigned so){
  if(NT==0) asm volatile("s_mov_b32 m0, %0\n\ts_nop 0\n\t"
    "buffer_load_dwordx4 %1, %5, %6 offen lds\n\t"
    "buffer_load_dwordx4 %2, %5, %6 offen offset:1024 lds\n\t"
    "buffer_load_dwordx4 %3, %5, %6 offen offset:2048 lds\n\t"
    "buffer_load_dwordx4 %4, %5, %6 offen offset:3072 lds"
    :: "s"(m0v),"v"(vo[0]),"v"(vo[1]),"v"(vo[2]),"v"(vo[3]),"s"(rs),"s"(so) : "memory");
  else asm volatile("s_mov_b32 m0, %0\n\ts_nop 0\n\t"
    "buffer_load_dwordx4 %1, %5, %6 offen nt lds\n\t"
    "buffer_load_dwordx4 %2, %5, %6 offen offset:1024 nt lds\n\t"
    "buffer_load_dwordx4 %3, %5, %6 offen offset:2048 nt lds\n\t"
    "buffer_load_dwordx4 %4, %5, %6 offen offset:3072 nt lds"
    :: "s"(m0v),"v"(vo[0]),"v"(vo[1]),"v"(vo[2]),"v"(vo[3]),"s"(rs),"s"(so) : "memory");
}
#define WAITV(n) asm volatile("s_waitcnt vmcnt(" #n ")":::"memory")
#define BAR() do{ asm volatile("s_waitcnt lgkmcnt(0)":::"memory"); __builtin_amdgcn_s_barrier(); asm volatile("":::"memory"); }while(0)

struct LaneC { unsigned aR[4], aT[2]; };
DI LaneC lane_consts(unsigned ldsb,int l,int w){
  LaneC c; const int h=l>>5, bp=w>>1, ch=w&1, r=l&31;
  for(int ks=0;ks<4;ks++) c.aR[ks]=ldsb+bp*8192+r*128+16*((2*ks+h)^((r>>1)&7));
  const int blk=(l>>4)&1,q=(l&15)>>2,p=l&3, sp=2*(p&1)+(p>>1);
  for(int ct=0;ct<2;ct++) c.aT[ct]=ldsb+16384+ch*8192+(8*h+q)*128+16*((4*ct+2*blk+(sp>>1))^(4*((q>>1)&1)))+8*(sp&1);
  return c;
}

template<int PHASE> DI void stage_mma(const LaneC& c, unsigned so, f32x16 (&acc)[2][2]){
  #pragma unroll
  for(int ks=0;ks<4;ks++){
    bf16x8 R0=lds128(c.aR[ks]+so), R1=lds128(c.aR[ks]+so+4096);
    bf16x8 T0=ldstr(c.aT[0]+so+ks*2048), T1=ldstr(c.aT[1]+so+ks*2048);
    if(PHASE==1){
      acc[0][0]=MFMA(T0,R0,acc[0][0]); acc[0][1]=MFMA(T0,R1,acc[0][1]); acc[1][0]=MFMA(T1,R0,acc[1][0]); acc[1][1]=MFMA(T1,R1,acc[1][1]);
    } else {
      acc[0][0]=MFMA(R0,T0,acc[0][0]); acc[0][1]=MFMA(R0,T1,acc[0][1]); acc[1][0]=MFMA(R1,T0,acc[1][0]); acc[1][1]=MFMA(R1,T1,acc[1][1]);
    }
  }
}

__global__ __launch_bounds__(256,1) void k1(Args a){
  extern __shared__ __attribute__((aligned(16))) char smem[];
  const int tid=threadIdx.x,l=tid&63; const int w=__builtin_amdgcn_readfirstlane(tid>>6);
  const int bid=blockIdx.x,g=bid&7,idx=bid>>3,br=g>>2,ksp=g&3,r=idx&3,hl=idx>>2,head=8*ksp+hl;
  const unsigned ldsb=(unsigned)(size_t)(LAS char*)smem;
  const LaneC c=lane_consts(ldsb,l,w);
  const __amdgpu_buffer_rsrc_t rx=__builtin_amdgcn_make_buffer_rsrc((void*)a.x[br],0,512*1024*2,0x00020000);
  const __amdgpu_buffer_rsrc_t rw=__builtin_amdgcn_make_buffer_rsrc((void*)a.wq[br],0,1024*4096*2,0x00020000);
  const __amdgpu_buffer_rsrc_t rk=__builtin_amdgcn_make_buffer_rsrc((void*)a.k[br],0,16*32*32*128*2,0x00020000);
  unsigned voR[4],voT[4];
  #pragma unroll
  for(int i=0;i<4;i++){
    int row=32*w+8*i+(l>>3); voR[i]=(r*128+row)*2048+16*((l&7)^((4*i+(l>>4))&7))-1024*i;
    int rg=4*(w&1)+i, krow=8*rg+(l>>3); voT[i]=krow*8192+head*256+(w>>1)*128+16*((l&7)^(4*((l>>4)&1)))-1024*i;
  }
  const int bp=w>>1,ch=w&1,h=l>>5;
  bf16x8 kf[2][2][2];
  #pragma unroll
  for(int b=0;b<2;b++) for(int ct=0;ct<2;ct++) for(int s=0;s<2;s++){
    int batch=4*r+2*bp+b; unsigned off=((batch*32+head)*32+(l&31))*256+ch*128+ct*64+s*32+16*h;
    kf[b][ct][s]=__builtin_bit_cast(bf16x8,__builtin_amdgcn_raw_buffer_load_b128(rk,off,0,POL_K?2:0)); }
  asm volatile("":: "v"(kf[0][0][0]),"v"(kf[0][0][1]),"v"(kf[0][1][0]),"v"(kf[0][1][1]),"v"(kf[1][0][0]),"v"(kf[1][0][1]),"v"(kf[1][1][0]),"v"(kf[1][1][1]));
  auto issue=[&](int u){ unsigned sb=ldsb+(u%NSLOT)*SLOT+w*4096; dma4<POL_X>(sb,voR,rx,u*128); dma4<POL_WQ>(sb+16384,voT,rw,u*524288); };
  issue(0);issue(1);issue(2);issue(3);
  f32x16 acc[2][2]={};
  #pragma unroll
  for(int u=0;u<16;u++){
    if(u<=12) WAITV(24); else if(u==13) WAITV(16); else if(u==14) WAITV(8); else WAITV(0);
    BAR();
    if(u+4<16) issue(u+4);
    stage_mma<1>(c,(u%NSLOT)*SLOT,acc);
  }
  f32x16 sp[2]={};
  #pragma unroll
  for(int b=0;b<2;b++) for(int ct=0;ct<2;ct++) for(int s=0;s<2;s++) sp[b]=MFMA(kf[b][ct][s],pack8(acc[ct][b],s),sp[b]);
  BAR();
  { const f32x16& o= ch? sp[0]:sp[1];
    #pragma unroll
    for(int i=0;i<4;i++){ u32x4 v={__float_as_uint(o[4*i]),__float_as_uint(o[4*i+1]),__float_as_uint(o[4*i+2]),__float_as_uint(o[4*i+3])}; *(LAS u32x4*)(size_t)(ldsb+w*4096+i*1024+l*16)=v; } }
  BAR();
  f32x16 S= ch? sp[1]:sp[0];
  #pragma unroll
  for(int i=0;i<4;i++){ u32x4 v=*(const LAS u32x4*)(size_t)(ldsb+(w^1)*4096+i*1024+l*16); for(int e=0;e<4;e++) S[4*i+e]+=__uint_as_float(v[e]); }
  float m=S[0];
  #pragma unroll
  for(int i=1;i<16;i++) m=fmaxf(m,S[i]);
  m=fmaxf(m,__shfl_xor(m,32));
  const float sc=0.08838834764831845f*1.4426950408889634f; float sum=0;
  #pragma unroll
  for(int i=0;i<16;i++){ S[i]=__builtin_amdgcn_exp2f((S[i]-m)*sc); sum+=S[i]; }
  sum+=__shfl_xor(sum,32);
  float inv=1.0f/sum;
  #pragma unroll
  for(int i=0;i<16;i++) S[i]*=inv;
  const int batch=4*r+2*bp+ch;
  char* pp=a.ws+WS_P+(size_t)((br*16+batch)*32+head)*2048+l*16;
  *(bf16x8*)pp=pack8(S,0); *(bf16x8*)(pp+1024)=pack8(S,1);
}

__global__ __launch_bounds__(256,1) void k2(Args a){
  extern __shared__ __attribute__((aligned(16))) char smem[];
  const int tid=threadIdx.x,l=tid&63; const int w=__builtin_amdgcn_readfirstlane(tid>>6);
  const int bid=blockIdx.x,g=bid&7,idx=bid>>3,br=g>>2,ksp=g&3,r=idx&3,j=idx>>2;
  const unsigned ldsb=(unsigned)(size_t)(LAS char*)smem;
  const LaneC c=lane_consts(ldsb,l,w);
  const __amdgpu_buffer_rsrc_t rv=__builtin_amdgcn_make_buffer_rsrc((void*)a.v[br],0,16*32*32*128*2,0x00020000);
  const __amdgpu_buffer_rsrc_t rw=__builtin_amdgcn_make_buffer_rsrc((void*)a.wo[br],0,4096*1024*2,0x00020000);
  const __amdgpu_buffer_rsrc_t rws=__builtin_amdgcn_make_buffer_rsrc((void*)a.ws,0,16u<<20,0x00020000);
  gu64* ep=(gu64*)(a.ws+WS_EPOCH+((br*4+r)*8+j)*256);
  const unsigned long long tag=__hip_atomic_load(ep,RLX_AGENT)+1ull;
  unsigned voR[4],voT[4];
  #pragma unroll
  for(int i=0;i<4;i++){
    int key=8*i+(l>>3); voR[i]=((4*r+w)*32*32+key)*256+16*((l&7)^((4*i+(l>>4))&7))-1024*i;
    int rg=4*(w&1)+i, krow=8*rg+(l>>3); voT[i]=krow*2048+j*256+(w>>1)*128+16*((l&7)^(4*((l>>4)&1)))-1024*i;
  }
  const int bp=w>>1,ch=w&1,h=l>>5;
  bf16x8 pf[2][8][2];
  #pragma unroll
  for(int b=0;b<2;b++) for(int hh=0;hh<8;hh++) for(int s=0;s<2;s++){
    unsigned off=WS_P+((br*16+4*r+2*bp+b)*32+8*ksp+hh)*2048+s*1024+l*16;
    pf[b][hh][s]=__builtin_bit_cast(bf16x8,__builtin_amdgcn_raw_buffer_load_b128(rws,off,0,0)); }
  #pragma unroll
  for(int b=0;b<2;b++) for(int hh=0;hh<8;hh+=4) asm volatile("":: "v"(pf[b][hh][0]),"v"(pf[b][hh][1]),"v"(pf[b][hh+1][0]),"v"(pf[b][hh+1][1]),"v"(pf[b][hh+2][0]),"v"(pf[b][hh+2][1]),"v"(pf[b][hh+3][0]),"v"(pf[b][hh+3][1]));
  auto issue=[&](int u){ unsigned sb=ldsb+(u%NSLOT)*SLOT+w*4096; dma4<POL_V>(sb,voR,rv,(8*ksp+(u>>1))*8192+(u&1)*128); dma4<POL_WO>(sb+16384,voT,rw,(ksp*1024+u*64)*2048); };
  issue(0);issue(1);issue(2);issue(3);
  f32x16 o[2][2]={};
  f32x16 acc[2][2];
  #pragma unroll
  for(int u=0;u<16;u++){
    if(u<=12) WAITV(24); else if(u==13) WAITV(16); else if(u==14) WAITV(8); else WAITV(0);
    BAR();
    if(u+4<16) issue(u+4);
    if((u&1)==0){ for(int b=0;b<2;b++) for(int ct=0;ct<2;ct++) acc[b][ct]=f32x16{}; }
    stage_mma<2>(c,(u%NSLOT)*SLOT,acc);
    if(u&1){
      #pragma unroll
      for(int b=0;b<2;b++) for(int ct=0;ct<2;ct++) for(int s=0;s<2;s++) o[b][ct]=MFMA(pack8(acc[b][ct],s),pf[b][u>>1][s],o[b][ct]);
    }
  }
  auto box=[&](int src_s,int dst_s)->unsigned{ int wg=((idx<<3)|(br*4+src_s)); return (unsigned)WS_PART+(((wg*4+w)*3)+((dst_s-src_s-1)&3))*2048; };
  #pragma unroll
  for(int u=0;u<4;u++){ if(u!=ksp){
      const f32x16& t=o[u>>1][u&1];
      __builtin_amdgcn_raw_buffer_store_b128(__builtin_bit_cast(u32x4,pack8(t,0)),rws,box(ksp,u)+l*16,0,16);
      __builtin_amdgcn_raw_buffer_store_b128(__builtin_bit_cast(u32x4,pack8(t,1)),rws,box(ksp,u)+1024+l*16,0,16); } }
  WAITV(0);
  gu64* flags=(gu64*)(a.ws+WS_FLAG);
  if(l==0) __hip_atomic_store(flags+(bid*4+w)*16,tag,RLX_AGENT);
  { int ss=(ksp+1+(l%3))&3; gu64* f=flags+((((idx<<3)|(br*4+ss))*4+w)*16);
    for(int spin=0;spin<(1<<22);spin++){ unsigned long long v=__hip_atomic_load(f,RLX_AGENT); if(__all(v==tag)) break; __builtin_amdgcn_s_sleep(1);} }
  asm volatile("":::"memory");
  f32x16 fin= ksp==0? o[0][0] : ksp==1? o[0][1] : ksp==2? o[1][0] : o[1][1];
  #pragma unroll
  for(int d=1;d<4;d++){ int ss=(ksp+d)&3;
    #pragma unroll
    for(int s=0;s<2;s++){ u32x4 v=__builtin_amdgcn_raw_buffer_load_b128(rws,box(ss,ksp)+s*1024+l*16,0,16);
      #pragma unroll
      for(int e=0;e<4;e++){ fin[8*s+2*e]+=__uint_as_float(v[e]<<16); fin[8*s+2*e+1]+=__uint_as_float(v[e]&0xffff0000u); } } }
  { const int batch=4*r+2*bp+(ksp>>1); char* op=(char*)a.out+(size_t)(batch*32+(l&31))*4096+br*2048+(j*128+ch*64+(ksp&1)*32+8*h)*2;
    *(bf16x8*)op=pack8(fin,0); *(bf16x8*)(op+32)=pack8(fin,1); }
  if(ksp==0&&tid==0) __hip_atomic_store(ep,tag,RLX_AGENT);
}

extern "C" void kernel_launch(void* const* d_in, const int* in_sizes, int n_in, void* d_out, int out_size, void* d_ws, size_t ws_size, hipStream_t stream){
  Args a;
  for(int b=0;b<2;b++){ a.x[b]=d_in[5*b]; a.k[b]=d_in[5*b+1]; a.v[b]=d_in[5*b+2]; a.wq[b]=d_in[5*b+3]; a.wo[b]=d_in[5*b+4]; }
  a.out=d_out; a.ws=(char*)d_ws;
  hipFuncSetAttribute((const void*)k1,hipFuncAttributeMaxDynamicSharedMemorySize,LDS_BYTES);
  hipFuncSetAttribute((const void*)k2,hipFuncAttributeMaxDynamicSharedMemorySize,LDS_BYTES);
  k1<<<256,256,LDS_BYTES,stream>>>(a);
  k2<<<256,256,LDS_BYTES,stream>>>(a);
}
```

```cpp
#define POL_X 0
#define POL_WQ 0
#define POL_K 0
#define POL_V 0
#define POL_WO 0
#include <hip/hip_runtime.h>
#include <hip/hip_bf16.h>
typedef __bf16 bf16x8 __attribute__((ext_vector_type(8)));
typedef __bf16 bf16x4 __attribute__((ext_vector_type(4)));
typedef __bf16 bf16x2 __attribute__((ext_vector_type(2)));
typedef float f32x16 __attribute__((ext_vector_type(16)));
typedef float f32x2 __attribute__((ext_vector_type(2)));
typedef unsigned u32x4 __attribute__((ext_vector_type(4)));
#define LAS __attribute__((address_space(3)))
#define GAS __attribute__((address_space(1)))
typedef GAS unsigned long long gu64;
#define DI __device__ __forceinline__
#define RLX_AGENT __ATOMIC_RELAXED, __HIP_MEMORY_SCOPE_AGENT
constexpr int SLOT=32768, NSLOT=5, LDS_BYTES=SLOT*NSLOT;
constexpr size_t WS_P=0, WS_PART=2u<<20, WS_FLAG=8u<<20, WS_EPOCH=WS_FLAG+(1u<<20);

struct Args { const void* x[2]; const void* k[2]; const void* v[2]; const void* wq[2]; const void* wo[2]; void* out; char* ws; };

DI unsigned cvtpk(float lo,float hi){ f32x2 v={lo,hi}; bf16x2 b=__builtin_convertvector(v,bf16x2); return __builtin_bit_cast(unsigned,b); }
DI bf16x8 pack8(const f32x16& a,int s){ u32x4 w; w[0]=cvtpk(a[8*s],a[8*s+1]); w[1]=cvtpk(a[8*s+2],a[8*s+3]); w[2]=cvtpk(a[8*s+4],a[8*s+5]); w[3]=cvtpk(a[8*s+6],a[8*s+7]); return __builtin_bit_cast(bf16x8,w); }
DI bf16x8 lds128(unsigned a){ return *(const LAS bf16x8*)(size_t)a; }
DI bf16x8 ldstr(unsigned a){ bf16x4 lo=__builtin_amdgcn_ds_read_tr16_b64_v4bf16((LAS bf16x4*)(size_t)a); bf16x4 hi=__builtin_amdgcn_ds_read_tr16_b64_v4bf16((LAS bf16x4*)(size_t)(a+512)); return __builtin_shufflevector(lo,hi,0,1,2,3,4,5,6,7); }
#define MFMA(a,b,c) __builtin_amdgcn_mfma_f32_32x32x16_bf16((a),(b),(c),0,0,0)

template<int NT> DI void dma4(unsigned m0v, const unsigned (&vo)[4], __amdgpu_buffer_rsrc_t rs, unsigned so){
  if(NT==0) asm volatile("s_mov_b32 m0, %0\n\ts_nop 0\n\t"
    "buffer_load_dwordx4 %1, %5, %6 offen lds\n\t"
    "buffer_load_dwordx4 %2, %5, %6 offen offset:1024 lds\n\t"
    "buffer_load_dwordx4 %3, %5, %6 offen offset:2048 lds\n\t"
    "buffer_load_dwordx4 %4, %5, %6 offen offset:3072 lds"
    :: "s"(m0v),"v"(vo[0]),"v"(vo[1]),"v"(vo[2]),"v"(vo[3]),"s"(rs),"s"(so) : "memory");
  else asm volatile("s_mov_b32 m0, %0\n\ts_nop 0\n\t"
    "buffer_load_dwordx4 %1, %5, %6 offen nt lds\n\t"
    "buffer_load_dwordx4 %2, %5, %6 offen offset:1024 nt lds\n\t"
    "buffer_load_dwordx4 %3, %5, %6 offen offset:2048 nt lds\n\t"
    "buffer_load_dwordx4 %4, %5, %6 offen offset:3072 nt lds"
    :: "s"(m0v),"v"(vo[0]),"v"(vo[1]),"v"(vo[2]),"v"(vo[3]),"s"(rs),"s"(so) : "memory");
}
#define WAITV(n) asm volatile("s_waitcnt vmcnt(" #n ")":::"memory")
#define BAR() do{ asm volatile("s_waitcnt lgkmcnt(0)":::"memory"); __builtin_amdgcn_s_barrier(); asm volatile("":::"memory"); }while(0)

struct LaneC { unsigned aR[4], aT[2]; };
DI LaneC lane_consts(unsigned ldsb,int l,int w){
  LaneC c; const int h=l>>5, bp=w>>1, ch=w&1, r=l&31;
  for(int ks=0;ks<4;ks++) c.aR[ks]=ldsb+bp*8192+r*128+16*((2*ks+h)^((r>>1)&7));
  const int blk=(l>>4)&1,q=(l&15)>>2,p=l&3, sp=2*(p&1)+(p>>1);
  for(int ct=0;ct<2;ct++) c.aT[ct]=ldsb+16384+ch*8192+(8*h+q)*128+16*((4*ct+2*blk+(sp>>1))^(4*((q>>1)&1)))+8*(sp&1);
  return c;
}

template<int PHASE> DI void stage_mma(const LaneC& c, unsigned so, f32x16 (&acc)[2][2]){
  #pragma unroll
  for(int ks=0;ks<4;ks++){
    bf16x8 R0=lds128(c.aR[ks]+so), R1=lds128(c.aR[ks]+so+4096);
    bf16x8 T0=ldstr(c.aT[0]+so+ks*2048), T1=ldstr(c.aT[1]+so+ks*2048);
    if(PHASE==1){
      acc[0][0]=MFMA(T0,R0,acc[0][0]); acc[0][1]=MFMA(T0,R1,acc[0][1]); acc[1][0]=MFMA(T1,R0,acc[1][0]); acc[1][1]=MFMA(T1,R1,acc[1][1]);
    } else {
      acc[0][0]=MFMA(R0,T0,acc[0][0]); acc[0][1]=MFMA(R0,T1,acc[0][1]); acc[1][0]=MFMA(R1,T0,acc[1][0]); acc[1][1]=MFMA(R1,T1,acc[1][1]);
    }
  }
}

__global__ __launch_bounds__(256,1) void kf(Args a){
  extern __shared__ __attribute__((aligned(16))) char smem[];
  const unsigned long long tstart=__builtin_amdgcn_s_memrealtime();
  gu64* gep=(gu64*)(a.ws+WS_EPOCH+65536); gu64* gfl=(gu64*)(a.ws+WS_EPOCH+131072);
  const unsigned long long gtag=__hip_atomic_load(gep,RLX_AGENT)+1ull;
  {

  const int tid=threadIdx.x,l=tid&63; const int w=__builtin_amdgcn_readfirstlane(tid>>6);
  const int bid=blockIdx.x,g=bid&7,idx=bid>>3,br=g>>2,ksp=g&3,r=idx&3,hl=idx>>2,head=8*ksp+hl;
    const unsigned ldsb=(unsigned)(size_t)(LAS char*)smem;
  const LaneC c=lane_consts(ldsb,l,w);
  const __amdgpu_buffer_rsrc_t rx=__builtin_amdgcn_make_buffer_rsrc((void*)a.x[br],0,512*1024*2,0x00020000);
  const __amdgpu_buffer_rsrc_t rw=__builtin_amdgcn_make_buffer_rsrc((void*)a.wq[br],0,1024*4096*2,0x00020000);
  const __amdgpu_buffer_rsrc_t rk=__builtin_amdgcn_make_buffer_rsrc((void*)a.k[br],0,16*32*32*128*2,0x00020000);
  unsigned voR[4],voT[4];
  #pragma unroll
  for(int i=0;i<4;i++){
    int row=32*w+8*i+(l>>3); voR[i]=(r*128+row)*2048+16*((l&7)^((4*i+(l>>4))&7))-1024*i;
    int rg=4*(w&1)+i, krow=8*rg+(l>>3); voT[i]=krow*8192+head*256+(w>>1)*128+16*((l&7)^(4*((l>>4)&1)))-1024*i;
  }
  const int bp=w>>1,ch=w&1,h=l>>5;
  bf16x8 kf[2][2][2];
  #pragma unroll
  for(int b=0;b<2;b++) for(int ct=0;ct<2;ct++) for(int s=0;s<2;s++){
    int batch=4*r+2*bp+b; unsigned off=((batch*32+head)*32+(l&31))*256+ch*128+ct*64+s*32+16*h;
    kf[b][ct][s]=__builtin_bit_cast(bf16x8,__builtin_amdgcn_raw_buffer_load_b128(rk,off,0,POL_K?2:0)); }
  asm volatile("":: "v"(kf[0][0][0]),"v"(kf[0][0][1]),"v"(kf[0][1][0]),"v"(kf[0][1][1]),"v"(kf[1][0][0]),"v"(kf[1][0][1]),"v"(kf[1][1][0]),"v"(kf[1][1][1]));
  auto issue=[&](int u){ unsigned sb=ldsb+(u%NSLOT)*SLOT+w*4096; dma4<POL_X>(sb,voR,rx,u*128); dma4<POL_WQ>(sb+16384,voT,rw,u*524288); };
  issue(0);issue(1);issue(2);issue(3);
  f32x16 acc[2][2]={};
  #pragma unroll
  for(int u=0;u<16;u++){
    if(u<=12) WAITV(24); else if(u==13) WAITV(16); else if(u==14) WAITV(8); else WAITV(0);
    BAR();
    if(u+4<16) issue(u+4);
    stage_mma<1>(c,(u%NSLOT)*SLOT,acc);
  }
  f32x16 sp[2]={};
  #pragma unroll
  for(int b=0;b<2;b++) for(int ct=0;ct<2;ct++) for(int s=0;s<2;s++) sp[b]=MFMA(kf[b][ct][s],pack8(acc[ct][b],s),sp[b]);
  BAR();
  { const f32x16& o= ch? sp[0]:sp[1];
    #pragma unroll
    for(int i=0;i<4;i++){ u32x4 v={__float_as_uint(o[4*i]),__float_as_uint(o[4*i+1]),__float_as_uint(o[4*i+2]),__float_as_uint(o[4*i+3])}; *(LAS u32x4*)(size_t)(ldsb+w*4096+i*1024+l*16)=v; } }
  BAR();
  f32x16 S= ch? sp[1]:sp[0];
  #pragma unroll
  for(int i=0;i<4;i++){ u32x4 v=*(const LAS u32x4*)(size_t)(ldsb+(w^1)*4096+i*1024+l*16); for(int e=0;e<4;e++) S[4*i+e]+=__uint_as_float(v[e]); }
  float m=S[0];
  #pragma unroll
  for(int i=1;i<16;i++) m=fmaxf(m,S[i]);
  m=fmaxf(m,__shfl_xor(m,32));
  const float sc=0.08838834764831845f*1.4426950408889634f; float sum=0;
  #pragma unroll
  for(int i=0;i<16;i++){ S[i]=__builtin_amdgcn_exp2f((S[i]-m)*sc); sum+=S[i]; }
  sum+=__shfl_xor(sum,32);
  float inv=1.0f/sum;
  #pragma unroll
  for(int i=0;i<16;i++) S[i]*=inv;
  const int batch=4*r+2*bp+ch;
  char* pp=a.ws+WS_P+(size_t)((br*16+batch)*32+head)*2048+l*16;
  *(bf16x8*)pp=pack8(S,0); *(bf16x8*)(pp+1024)=pack8(S,1);

}
  __threadfence(); __syncthreads();
  if(threadIdx.x==0) __hip_atomic_store(gfl+blockIdx.x*8,gtag,RLX_AGENT);
  { gu64* f=gfl+threadIdx.x*8; for(int spin=0;spin<(1<<22);spin++){ unsigned long long v=__hip_atomic_load(f,RLX_AGENT); if(v==gtag) break; __builtin_amdgcn_s_sleep(2);} }
  __syncthreads(); __threadfence();
  {

  const int tid=threadIdx.x,l=tid&63; const int w=__builtin_amdgcn_readfirstlane(tid>>6);
  const int bid=blockIdx.x,g=bid&7,idx=bid>>3,br=g>>2,ksp=g&3,r=idx&3,j=idx>>2;
    const unsigned ldsb=(unsigned)(size_t)(LAS char*)smem;
  const LaneC c=lane_consts(ldsb,l,w);
  const __amdgpu_buffer_rsrc_t rv=__builtin_amdgcn_make_buffer_rsrc((void*)a.v[br],0,16*32*32*128*2,0x00020000);
  const __amdgpu_buffer_rsrc_t rw=__builtin_amdgcn_make_buffer_rsrc((void*)a.wo[br],0,4096*1024*2,0x00020000);
  const __amdgpu_buffer_rsrc_t rws=__builtin_amdgcn_make_buffer_rsrc((void*)a.ws,0,16u<<20,0x00020000);
  gu64* ep=(gu64*)(a.ws+WS_EPOCH+((br*4+r)*8+j)*256);
  const unsigned long long tag=__hip_atomic_load(ep,RLX_AGENT)+1ull;
  unsigned voR[4],voT[4];
  #pragma unroll
  for(int i=0;i<4;i++){
    int key=8*i+(l>>3); voR[i]=((4*r+w)*32*32+key)*256+16*((l&7)^((4*i+(l>>4))&7))-1024*i;
    int rg=4*(w&1)+i, krow=8*rg+(l>>3); voT[i]=krow*2048+j*256+(w>>1)*128+16*((l&7)^(4*((l>>4)&1)))-1024*i;
  }
  const int bp=w>>1,ch=w&1,h=l>>5;
  bf16x8 pf[2][8][2];
  #pragma unroll
  for(int b=0;b<2;b++) for(int hh=0;hh<8;hh++) for(int s=0;s<2;s++){
    unsigned off=WS_P+((br*16+4*r+2*bp+b)*32+8*ksp+hh)*2048+s*1024+l*16;
    pf[b][hh][s]=__builtin_bit_cast(bf16x8,__builtin_amdgcn_raw_buffer_load_b128(rws,off,0,0)); }
  #pragma unroll
  for(int b=0;b<2;b++) for(int hh=0;hh<8;hh+=4) asm volatile("":: "v"(pf[b][hh][0]),"v"(pf[b][hh][1]),"v"(pf[b][hh+1][0]),"v"(pf[b][hh+1][1]),"v"(pf[b][hh+2][0]),"v"(pf[b][hh+2][1]),"v"(pf[b][hh+3][0]),"v"(pf[b][hh+3][1]));
  auto issue=[&](int u){ unsigned sb=ldsb+(u%NSLOT)*SLOT+w*4096; dma4<POL_V>(sb,voR,rv,(8*ksp+(u>>1))*8192+(u&1)*128); dma4<POL_WO>(sb+16384,voT,rw,(ksp*1024+u*64)*2048); };
  issue(0);issue(1);issue(2);issue(3);
  f32x16 o[2][2]={};
  f32x16 acc[2][2];
  #pragma unroll
  for(int u=0;u<16;u++){
    if(u<=12) WAITV(24); else if(u==13) WAITV(16); else if(u==14) WAITV(8); else WAITV(0);
    BAR();
    if(u+4<16) issue(u+4);
    if((u&1)==0){ for(int b=0;b<2;b++) for(int ct=0;ct<2;ct++) acc[b][ct]=f32x16{}; }
    stage_mma<2>(c,(u%NSLOT)*SLOT,acc);
    if(u&1){
      #pragma unroll
      for(int b=0;b<2;b++) for(int ct=0;ct<2;ct++) for(int s=0;s<2;s++) o[b][ct]=MFMA(pack8(acc[b][ct],s),pf[b][u>>1][s],o[b][ct]);
    }
  }
  auto box=[&](int src_s,int dst_s)->unsigned{ int wg=((idx<<3)|(br*4+src_s)); return (unsigned)WS_PART+(((wg*4+w)*3)+((dst_s-src_s-1)&3))*2048; };
  #pragma unroll
  for(int u=0;u<4;u++){ if(u!=ksp){
      const f32x16& t=o[u>>1][u&1];
      __builtin_amdgcn_raw_buffer_store_b128(__builtin_bit_cast(u32x4,pack8(t,0)),rws,box(ksp,u)+l*16,0,16);
      __builtin_amdgcn_raw_buffer_store_b128(__builtin_bit_cast(u32x4,pack8(t,1)),rws,box(ksp,u)+1024+l*16,0,16); } }
  WAITV(0);
  gu64* flags=(gu64*)(a.ws+WS_FLAG);
  if(l==0) __hip_atomic_store(flags+(bid*4+w)*16,tag,RLX_AGENT);
  { int ss=(ksp+1+(l%3))&3; gu64* f=flags+((((idx<<3)|(br*4+ss))*4+w)*16);
    for(int spin=0;spin<(1<<22);spin++){ unsigned long long v=__hip_atomic_load(f,RLX_AGENT); if(__all(v==tag)) break; __builtin_amdgcn_s_sleep(1);} }
  asm volatile("":::"memory");
  f32x16 fin= ksp==0? o[0][0] : ksp==1? o[0][1] : ksp==2? o[1][0] : o[1][1];
  #pragma unroll
  for(int d=1;d<4;d++){ int ss=(ksp+d)&3;
    #pragma unroll
    for(int s=0;s<2;s++){ u32x4 v=__builtin_amdgcn_raw_buffer_load_b128(rws,box(ss,ksp)+s*1024+l*16,0,16);
      #pragma unroll
      for(int e=0;e<4;e++){ fin[8*s+2*e]+=__uint_as_float(v[e]<<16); fin[8*s+2*e+1]+=__uint_as_float(v[e]&0xffff0000u); } } }
  { const int batch=4*r+2*bp+(ksp>>1); char* op=(char*)a.out+(size_t)(batch*32+(l&31))*4096+br*2048+(j*128+ch*64+(ksp&1)*32+8*h)*2;
    *(bf16x8*)op=pack8(fin,0); *(bf16x8*)(op+32)=pack8(fin,1); }
  while(__builtin_amdgcn_s_memrealtime()<tstart+10000) __builtin_amdgcn_s_sleep(1);
  if(ksp==0&&tid==0) __hip_atomic_store(ep,tag,RLX_AGENT);
}
  if(blockIdx.x==0&&threadIdx.x==0) __hip_atomic_store(gep,gtag,RLX_AGENT);
}
extern "C" void kernel_launch(void* const* d_in, const int* in_sizes, int n_in, void* d_out, int out_size, void* d_ws, size_t ws_size, hipStream_t stream){
  Args a;
  for(int b=0;b<2;b++){ a.x[b]=d_in[5*b]; a.k[b]=d_in[5*b+1]; a.v[b]=d_in[5*b+2]; a.wq[b]=d_in[5*b+3]; a.wo[b]=d_in[5*b+4]; }
  a.out=d_out; a.ws=(char*)d_ws;
  hipFuncSetAttribute((const void*)kf,hipFuncAttributeMaxDynamicSharedMemorySize,LDS_BYTES);
  kf<<<256,256,LDS_BYTES,stream>>>(a);
}
```

```cpp
#define POL_X 0
#define POL_WQ 0
#define POL_K 0
#define POL_V 0
#define POL_WO 0
#include <hip/hip_runtime.h>
#include <hip/hip_bf16.h>
typedef __bf16 bf16x8 __attribute__((ext_vector_type(8)));
typedef __bf16 bf16x4 __attribute__((ext_vector_type(4)));
typedef __bf16 bf16x2 __attribute__((ext_vector_type(2)));
typedef float f32x16 __attribute__((ext_vector_type(16)));
typedef float f32x2 __attribute__((ext_vector_type(2)));
typedef unsigned u32x4 __attribute__((ext_vector_type(4)));
#define LAS __attribute__((address_space(3)))
#define GAS __attribute__((address_space(1)))
typedef GAS unsigned long long gu64;
#define DI __device__ __forceinline__
#define RLX_AGENT __ATOMIC_RELAXED, __HIP_MEMORY_SCOPE_AGENT
constexpr int SLOT=32768, NSLOT=2, LDS_BYTES=SLOT*NSLOT;
constexpr size_t WS_P=0, WS_PART=2u<<20, WS_FLAG=8u<<20, WS_EPOCH=WS_FLAG+(1u<<20);

struct Args { const void* x[2]; const void* k[2]; const void* v[2]; const void* wq[2]; const void* wo[2]; void* out; char* ws; };

DI unsigned cvtpk(float lo,float hi){ f32x2 v={lo,hi}; bf16x2 b=__builtin_convertvector(v,bf16x2); return __builtin_bit_cast(unsigned,b); }
DI bf16x8 pack8(const f32x16& a,int s){ u32x4 w; w[0]=cvtpk(a[8*s],a[8*s+1]); w[1]=cvtpk(a[8*s+2],a[8*s+3]); w[2]=cvtpk(a[8*s+4],a[8*s+5]); w[3]=cvtpk(a[8*s+6],a[8*s+7]); return __builtin_bit_cast(bf16x8,w); }
DI bf16x8 lds128(unsigned a){ return *(const LAS bf16x8*)(size_t)a; }
DI bf16x8 ldstr(unsigned a){ bf16x4 lo=__builtin_amdgcn_ds_read_tr16_b64_v4bf16((LAS bf16x4*)(size_t)a); bf16x4 hi=__builtin_amdgcn_ds_read_tr16_b64_v4bf16((LAS bf16x4*)(size_t)(a+512)); return __builtin_shufflevector(lo,hi,0,1,2,3,4,5,6,7); }
#define MFMA(a,b,c) __builtin_amdgcn_mfma_f32_32x32x16_bf16((a),(b),(c),0,0,0)

template<int NT> DI void dma4(unsigned m0v, const unsigned (&vo)[4], __amdgpu_buffer_rsrc_t rs, unsigned so){
  if(NT==0) asm volatile("s_mov_b32 m0, %0\n\ts_nop 0\n\t"
    "buffer_load_dwordx4 %1, %5, %6 offen lds\n\t"
    "buffer_load_dwordx4 %2, %5, %6 offen offset:1024 lds\n\t"
    "buffer_load_dwordx4 %3, %5, %6 offen offset:2048 lds\n\t"
    "buffer_load_dwordx4 %4, %5, %6 offen offset:3072 lds"
    :: "s"(m0v),"v"(vo[0]),"v"(vo[1]),"v"(vo[2]),"v"(vo[3]),"s"(rs),"s"(so) : "memory");
  else asm volatile("s_mov_b32 m0, %0\n\ts_nop 0\n\t"
    "buffer_load_dwordx4 %1, %5, %6 offen nt lds\n\t"
    "buffer_load_dwordx4 %2, %5, %6 offen offset:1024 nt lds\n\t"
    "buffer_load_dwordx4 %3, %5, %6 offen offset:2048 nt lds\n\t"
    "buffer_load_dwordx4 %4, %5, %6 offen offset:3072 nt lds"
    :: "s"(m0v),"v"(vo[0]),"v"(vo[1]),"v"(vo[2]),"v"(vo[3]),"s"(rs),"s"(so) : "memory");
}
#define WAITV(n) asm volatile("s_waitcnt vmcnt(" #n ")":::"memory")
#define BAR() do{ asm volatile("s_waitcnt lgkmcnt(0)":::"memory"); __builtin_amdgcn_s_barrier(); asm volatile("":::"memory"); }while(0)

struct LaneC { unsigned aR[4], aT[2]; };
DI LaneC lane_consts(unsigned ldsb,int l,int w){
  LaneC c; const int h=l>>5, bp=w>>1, ch=w&1, r=l&31;
  for(int ks=0;ks<4;ks++) c.aR[ks]=ldsb+bp*8192+r*128+16*((2*ks+h)^((r>>1)&7));
  const int blk=(l>>4)&1,q=(l&15)>>2,p=l&3, sp=2*(p&1)+(p>>1);
  for(int ct=0;ct<2;ct++) c.aT[ct]=ldsb+16384+ch*8192+(8*h+q)*128+16*((4*ct+2*blk+(sp>>1))^(4*((q>>1)&1)))+8*(sp&1);
  return c;
}

template<int PHASE> DI void stage_mma(const LaneC& c, unsigned so, f32x16 (&acc)[2][2]){
  #pragma unroll
  for(int ks=0;ks<4;ks++){
    bf16x8 R0=lds128(c.aR[ks]+so), R1=lds128(c.aR[ks]+so+4096);
    bf16x8 T0=ldstr(c.aT[0]+so+ks*2048), T1=ldstr(c.aT[1]+so+ks*2048);
    if(PHASE==1){
      acc[0][0]=MFMA(T0,R0,acc[0][0]); acc[0][1]=MFMA(T0,R1,acc[0][1]); acc[1][0]=MFMA(T1,R0,acc[1][0]); acc[1][1]=MFMA(T1,R1,acc[1][1]);
    } else {
      acc[0][0]=MFMA(R0,T0,acc[0][0]); acc[0][1]=MFMA(R0,T1,acc[0][1]); acc[1][0]=MFMA(R1,T0,acc[1][0]); acc[1][1]=MFMA(R1,T1,acc[1][1]);
    }
  }
}

__global__ __launch_bounds__(256,1) void kf(Args a){
  extern __shared__ __attribute__((aligned(16))) char smem[];
  const unsigned long long tstart=__builtin_amdgcn_s_memrealtime();
  gu64* gep=(gu64*)(a.ws+WS_EPOCH+65536); gu64* gfl=(gu64*)(a.ws+WS_EPOCH+131072);
  const unsigned long long gtag=__hip_atomic_load(gep,RLX_AGENT)+1ull;
  {

  const int tid=threadIdx.x,l=tid&63; const int w=__builtin_amdgcn_readfirstlane(tid>>6);
  const int bid=blockIdx.x,g=bid&7,idx=bid>>3,br=g>>2,ksp=g&3,r=idx&3,hl=idx>>2,head=8*ksp+hl;
    const unsigned ldsb=(unsigned)(size_t)(LAS char*)smem;
  const LaneC c=lane_consts(ldsb,l,w);
  const __amdgpu_buffer_rsrc_t rx=__builtin_amdgcn_make_buffer_rsrc((void*)a.x[br],0,512*1024*2,0x00020000);
  const __amdgpu_buffer_rsrc_t rw=__builtin_amdgcn_make_buffer_rsrc((void*)a.wq[br],0,1024*4096*2,0x00020000);
  const __amdgpu_buffer_rsrc_t rk=__builtin_amdgcn_make_buffer_rsrc((void*)a.k[br],0,16*32*32*128*2,0x00020000);
  unsigned voR[4],voT[4];
  #pragma unroll
  for(int i=0;i<4;i++){
    int row=32*w+8*i+(l>>3); voR[i]=(r*128+row)*2048+16*((l&7)^((4*i+(l>>4))&7))-1024*i;
    int rg=4*(w&1)+i, krow=8*rg+(l>>3); voT[i]=krow*8192+head*256+(w>>1)*128+16*((l&7)^(4*((l>>4)&1)))-1024*i;
  }
  const int bp=w>>1,ch=w&1,h=l>>5;
  bf16x8 kf[2][2][2];
  #pragma unroll
  for(int b=0;b<2;b++) for(int ct=0;ct<2;ct++) for(int s=0;s<2;s++){
    int batch=4*r+2*bp+b; unsigned off=((batch*32+head)*32+(l&31))*256+ch*128+ct*64+s*32+16*h;
    kf[b][ct][s]=__builtin_bit_cast(bf16x8,__builtin_amdgcn_raw_buffer_load_b128(rk,off,0,POL_K?2:0)); }
  asm volatile("":: "v"(kf[0][0][0]),"v"(kf[0][0][1]),"v"(kf[0][1][0]),"v"(kf[0][1][1]),"v"(kf[1][0][0]),"v"(kf[1][0][1]),"v"(kf[1][1][0]),"v"(kf[1][1][1]));
  auto issue=[&](int u){ unsigned sb=ldsb+(u%NSLOT)*SLOT+w*4096; dma4<POL_X>(sb,voR,rx,u*128); dma4<POL_WQ>(sb+16384,voT,rw,u*524288); };
  issue(0);
  f32x16 acc[2][2]={};
  #pragma unroll
  for(int u=0;u<16;u++){
    WAITV(0);
    BAR();
    if(u+1<16) issue(u+1);
    stage_mma<1>(c,(u%NSLOT)*SLOT,acc);
  }
  f32x16 sp[2]={};
  #pragma unroll
  for(int b=0;b<2;b++) for(int ct=0;ct<2;ct++) for(int s=0;s<2;s++) sp[b]=MFMA(kf[b][ct][s],pack8(acc[ct][b],s),sp[b]);
  BAR();
  { const f32x16& o= ch? sp[0]:sp[1];
    #pragma unroll
    for(int i=0;i<4;i++){ u32x4 v={__float_as_uint(o[4*i]),__float_as_uint(o[4*i+1]),__float_as_uint(o[4*i+2]),__float_as_uint(o[4*i+3])}; *(LAS u32x4*)(size_t)(ldsb+w*4096+i*1024+l*16)=v; } }
  BAR();
  f32x16 S= ch? sp[1]:sp[0];
  #pragma unroll
  for(int i=0;i<4;i++){ u32x4 v=*(const LAS u32x4*)(size_t)(ldsb+(w^1)*4096+i*1024+l*16); for(int e=0;e<4;e++) S[4*i+e]+=__uint_as_float(v[e]); }
  float m=S[0];
  #pragma unroll
  for(int i=1;i<16;i++) m=fmaxf(m,S[i]);
  m=fmaxf(m,__shfl_xor(m,32));
  const float sc=0.08838834764831845f*1.4426950408889634f; float sum=0;
  #pragma unroll
  for(int i=0;i<16;i++){ S[i]=__builtin_amdgcn_exp2f((S[i]-m)*sc); sum+=S[i]; }
  sum+=__shfl_xor(sum,32);
  float inv=1.0f/sum;
  #pragma unroll
  for(int i=0;i<16;i++) S[i]*=inv;
  const int batch=4*r+2*bp+ch;
  char* pp=a.ws+WS_P+(size_t)((br*16+batch)*32+head)*2048+l*16;
  *(bf16x8*)pp=pack8(S,0); *(bf16x8*)(pp+1024)=pack8(S,1);

}
  __threadfence(); __syncthreads();
  if(threadIdx.x==0) __hip_atomic_store(gfl+blockIdx.x*8,gtag,RLX_AGENT);
  { gu64* f=gfl+threadIdx.x*8; for(int spin=0;spin<(1<<22);spin++){ unsigned long long v=__hip_atomic_load(f,RLX_AGENT); if(v==gtag) break; __builtin_amdgcn_s_sleep(2);} }
  __syncthreads(); __threadfence();
  {

  const int tid=threadIdx.x,l=tid&63; const int w=__builtin_amdgcn_readfirstlane(tid>>6);
  const int bid=blockIdx.x,g=bid&7,idx=bid>>3,br=g>>2,ksp=g&3,r=idx&3,j=idx>>2;
    const unsigned ldsb=(unsigned)(size_t)(LAS char*)smem;
  const LaneC c=lane_consts(ldsb,l,w);
  const __amdgpu_buffer_rsrc_t rv=__builtin_amdgcn_make_buffer_rsrc((void*)a.v[br],0,16*32*32*128*2,0x00020000);
  const __amdgpu_buffer_rsrc_t rw=__builtin_amdgcn_make_buffer_rsrc((void*)a.wo[br],0,4096*1024*2,0x00020000);
  const __amdgpu_buffer_rsrc_t rws=__builtin_amdgcn_make_buffer_rsrc((void*)a.ws,0,16u<<20,0x00020000);
  gu64* ep=(gu64*)(a.ws+WS_EPOCH+((br*4+r)*8+j)*256);
  const unsigned long long tag=__hip_atomic_load(ep,RLX_AGENT)+1ull;
  unsigned voR[4],voT[4];
  #pragma unroll
  for(int i=0;i<4;i++){
    int key=8*i+(l>>3); voR[i]=((4*r+w)*32*32+key)*256+16*((l&7)^((4*i+(l>>4))&7))-1024*i;
    int rg=4*(w&1)+i, krow=8*rg+(l>>3); voT[i]=krow*2048+j*256+(w>>1)*128+16*((l&7)^(4*((l>>4)&1)))-1024*i;
  }
  const int bp=w>>1,ch=w&1,h=l>>5;
  bf16x8 pf[2][8][2];
  #pragma unroll
  for(int b=0;b<2;b++) for(int hh=0;hh<8;hh++) for(int s=0;s<2;s++){
    unsigned off=WS_P+((br*16+4*r+2*bp+b)*32+8*ksp+hh)*2048+s*1024+l*16;
    pf[b][hh][s]=__builtin_bit_cast(bf16x8,__builtin_amdgcn_raw_buffer_load_b128(rws,off,0,0)); }
  #pragma unroll
  for(int b=0;b<2;b++) for(int hh=0;hh<8;hh+=4) asm volatile("":: "v"(pf[b][hh][0]),"v"(pf[b][hh][1]),"v"(pf[b][hh+1][0]),"v"(pf[b][hh+1][1]),"v"(pf[b][hh+2][0]),"v"(pf[b][hh+2][1]),"v"(pf[b][hh+3][0]),"v"(pf[b][hh+3][1]));
  auto issue=[&](int u){ unsigned sb=ldsb+(u%NSLOT)*SLOT+w*4096; dma4<POL_V>(sb,voR,rv,(8*ksp+(u>>1))*8192+(u&1)*128); dma4<POL_WO>(sb+16384,voT,rw,(ksp*1024+u*64)*2048); };
  issue(0);
  f32x16 o[2][2]={};
  f32x16 acc[2][2];
  #pragma unroll
  for(int u=0;u<16;u++){
    WAITV(0);
    BAR();
    if(u+1<16) issue(u+1);
    if((u&1)==0){ for(int b=0;b<2;b++) for(int ct=0;ct<2;ct++) acc[b][ct]=f32x16{}; }
    stage_mma<2>(c,(u%NSLOT)*SLOT,acc);
    if(u&1){
      #pragma unroll
      for(int b=0;b<2;b++) for(int ct=0;ct<2;ct++) for(int s=0;s<2;s++) o[b][ct]=MFMA(pack8(acc[b][ct],s),pf[b][u>>1][s],o[b][ct]);
    }
  }
  auto box=[&](int src_s,int dst_s)->unsigned{ int wg=((idx<<3)|(br*4+src_s)); return (unsigned)WS_PART+(((wg*4+w)*3)+((dst_s-src_s-1)&3))*2048; };
  #pragma unroll
  for(int u=0;u<4;u++){ if(u!=ksp){
      const f32x16& t=o[u>>1][u&1];
      __builtin_amdgcn_raw_buffer_store_b128(__builtin_bit_cast(u32x4,pack8(t,0)),rws,box(ksp,u)+l*16,0,16);
      __builtin_amdgcn_raw_buffer_store_b128(__builtin_bit_cast(u32x4,pack8(t,1)),rws,box(ksp,u)+1024+l*16,0,16); } }
  WAITV(0);
  gu64* flags=(gu64*)(a.ws+WS_FLAG);
  if(l==0) __hip_atomic_store(flags+(bid*4+w)*16,tag,RLX_AGENT);
  { int ss=(ksp+1+(l%3))&3; gu64* f=flags+((((idx<<3)|(br*4+ss))*4+w)*16);
    for(int spin=0;spin<(1<<22);spin++){ unsigned long long v=__hip_atomic_load(f,RLX_AGENT); if(__all(v==tag)) break; __builtin_amdgcn_s_sleep(1);} }
  asm volatile("":::"memory");
  f32x16 fin= ksp==0? o[0][0] : ksp==1? o[0][1] : ksp==2? o[1][0] : o[1][1];
  #pragma unroll
  for(int d=1;d<4;d++){ int ss=(ksp+d)&3;
    #pragma unroll
    for(int s=0;s<2;s++){ u32x4 v=__builtin_amdgcn_raw_buffer_load_b128(rws,box(ss,ksp)+s*1024+l*16,0,16);
      #pragma unroll
      for(int e=0;e<4;e++){ fin[8*s+2*e]+=__uint_as_float(v[e]<<16); fin[8*s+2*e+1]+=__uint_as_float(v[e]&0xffff0000u); } } }
  { const int batch=4*r+2*bp+(ksp>>1); char* op=(char*)a.out+(size_t)(batch*32+(l&31))*4096+br*2048+(j*128+ch*64+(ksp&1)*32+8*h)*2;
    *(bf16x8*)op=pack8(fin,0); *(bf16x8*)(op+32)=pack8(fin,1); }
  while(__builtin_amdgcn_s_memrealtime()<tstart+10000) __builtin_amdgcn_s_sleep(1);
  if(ksp==0&&tid==0) __hip_atomic_store(ep,tag,RLX_AGENT);
}
  if(blockIdx.x==0&&threadIdx.x==0) __hip_atomic_store(gep,gtag,RLX_AGENT);
}
extern "C" void kernel_launch(void* const* d_in, const int* in_sizes, int n_in, void* d_out, int out_size, void* d_ws, size_t ws_size, hipStream_t stream){
  Args a;
  for(int b=0;b<2;b++){ a.x[b]=d_in[5*b]; a.k[b]=d_in[5*b+1]; a.v[b]=d_in[5*b+2]; a.wq[b]=d_in[5*b+3]; a.wo[b]=d_in[5*b+4]; }
  a.out=d_out; a.ws=(char*)d_ws;
  hipFuncSetAttribute((const void*)kf,hipFuncAttributeMaxDynamicSharedMemorySize,LDS_BYTES);
  kf<<<256,256,LDS_BYTES,stream>>>(a);
}
```

```cpp
#include <hip/hip_runtime.h>
#include <hip/hip_bf16.h>
typedef unsigned u32x16 __attribute__((ext_vector_type(16)));
typedef unsigned u32x8 __attribute__((ext_vector_type(8)));
typedef unsigned u32x4 __attribute__((ext_vector_type(4)));
typedef unsigned u32x2 __attribute__((ext_vector_type(2)));
#define LAS __attribute__((address_space(3)))
constexpr int LDS_BYTES = 163840;
constexpr unsigned GRAN = 2u;
constexpr unsigned WS_P = 0u, WS_PART = 2u << 20, WS_FLAGP = 12u << 20, WS_FLAGR = (12u << 20) + (256u << 10), WS_EP = (12u << 20) + (512u << 10),
                   WS_ER = (12u << 20) + (576u << 10), WS_TS = 13u << 20;

__device__ __forceinline__ unsigned rfl(unsigned x) { return __builtin_amdgcn_readfirstlane(x); }

__global__ __launch_bounds__(256, 1) void fused_attn_pair(const void* x0, const void* k0, const void* v0, const void* wq0, const void* wo0,
                                                          const void* x1, const void* k1, const void* v1, const void* wq1, const void* wo1,
                                                          void* out, void* ws) {
  extern __shared__ __attribute__((aligned(16))) char smem[];
  u32x16 ka0; u32x8 ka1;
  asm volatile("s_load_dwordx16 %0, %2, 0x0\n\ts_load_dwordx8 %1, %2, 0x40" : "={s[64:79]}"(ka0), "={s[80:87]}"(ka1) : "s"(__builtin_amdgcn_kernarg_segment_ptr()) : "memory");
  unsigned tid = threadIdx.x;
  const unsigned bid = blockIdx.x, g = bid & 7u, idx = bid >> 3, br = g >> 2, ksp = g & 3u, r = idx & 3u, jh = idx >> 2;
  const unsigned head = 8u * ksp + jh;
  const unsigned j2 = jh ^ 2u;
  const unsigned kap = 3u ^ ksp, f1 = kap >> 1, f0 = kap & 1u;
  const unsigned ldsb = (unsigned)(size_t)(LAS char*)smem;
  const unsigned w = rfl(tid >> 6);
  const unsigned batch_w = 4u * r + (w ^ f1);
  u32x4 voX, voWq, sx, swq, sk, sv, swo, sws, sout;
  {
    const unsigned l = tid & 63u;
#pragma unroll
    for (unsigned i = 0; i < 4; ++i) {
      const unsigned swR = 16u * ((l & 7u) ^ ((4u * i + (l >> 4)) & 7u));
      const unsigned swT = 16u * ((l & 7u) ^ (4u * ((l >> 4) & 1u)) ^ (2u * (i & 1u)) ^ (4u * f0));
      voX[i] = (batch_w * 32u + 8u * i + (l >> 3)) * 2048u + swR - 1024u * i;
      voWq[i] = (8u * (4u * (w & 1u) + i) + (l >> 3)) * 8192u + head * 256u + (w >> 1) * 128u + swT - 1024u * i;
    }
  }
  const unsigned cls = (r + jh) & 3u;
  u32x2 perm1; perm1[0] = cls == 0u ? 0x69432510u : cls == 1u ? 0xa5436120u : cls == 2u ? 0x65472130u : 0x65832140u;
  perm1[1] = cls == 0u ? 0xfecbad87u : cls == 1u ? 0xfdcbe987u : cls == 2u ? 0xedcfa98bu : 0xfedba9c7u;
  const unsigned cls2 = (r + j2) & 3u;
  const unsigned perm2 = cls2 == 0u ? 0x76432510u : cls2 == 1u ? 0x75436120u : cls2 == 2u ? 0x65472130u : 0x76532140u;
  const unsigned sM0 = rfl(ldsb + w * 4096u);
  asm volatile(
    "s_waitcnt lgkmcnt(0)\n"
    "s_cmp_eq_u32 s48, 0\n"
    "s_cselect_b64 s[8:9], s[64:65], s[74:75]\n"
    "s_mov_b32 s10, 1048576\n"
    "s_mov_b32 s11, 0x20000\n"
    "s_cselect_b64 s[12:13], s[70:71], s[80:81]\n"
    "s_mov_b32 s14, 8388608\n"
    "s_mov_b32 s15, 0x20000\n"
    "s_cselect_b64 s[16:17], s[66:67], s[76:77]\n"
    "s_mov_b32 s18, 4194304\n"
    "s_mov_b32 s19, 0x20000\n"
    "s_cselect_b64 s[20:21], s[68:69], s[78:79]\n"
    "s_mov_b32 s22, 4194304\n"
    "s_mov_b32 s23, 0x20000\n"
    "s_cselect_b64 s[24:25], s[72:73], s[82:83]\n"
    "s_mov_b32 s26, 8388608\n"
    "s_mov_b32 s27, 0x20000\n"
    "s_and_b32 s9, s9, 0xffff\n"
    "s_and_b32 s13, s13, 0xffff\n"
    "s_add_u32 m0, s36, 0\n"
    "s_bfe_u32 s38, s56, 0x40000\n"
    "s_lshl_b32 s38, s38, 7\n"
    "buffer_load_dwordx4 v216, s[8:11], s38 offen lds\n"
    "buffer_load_dwordx4 v217, s[8:11], s38 offen offset:1024 lds\n"
    "buffer_load_dwordx4 v218, s[8:11], s38 offen offset:2048 lds\n"
    "buffer_load_dwordx4 v219, s[8:11], s38 offen offset:3072 lds\n"
    "s_add_u32 m0, s36, 16384\n"
    "s_bfe_u32 s38, s56, 0x40000\n"
    "s_lshl_b32 s38, s38, 19\n"
    "buffer_load_dwordx4 v220, s[12:15], s38 offen lds\n"
    "buffer_load_dwordx4 v221, s[12:15], s38 offen offset:1024 lds\n"
    "buffer_load_dwordx4 v222, s[12:15], s38 offen offset:2048 lds\n"
    "buffer_load_dwordx4 v223, s[12:15], s38 offen offset:3072 lds\n"
    "s_add_u32 m0, s36, 32768\n"
    "s_bfe_u32 s38, s56, 0x40004\n"
    "s_lshl_b32 s38, s38, 7\n"
    "buffer_load_dwordx4 v216, s[8:11], s38 offen lds\n"
    "buffer_load_dwordx4 v217, s[8:11], s38 offen offset:1024 lds\n"
    "buffer_load_dwordx4 v218, s[8:11], s38 offen offset:2048 lds\n"
    "buffer_load_dwordx4 v219, s[8:11], s38 offen offset:3072 lds\n"
    "s_add_u32 m0, s36, 49152\n"
    "s_bfe_u32 s38, s56, 0x40004\n"
    "s_lshl_b32 s38, s38, 19\n"
    "buffer_load_dwordx4 v220, s[12:15], s38 offen lds\n"
    "buffer_load_dwordx4 v221, s[12:15], s38 offen offset:1024 lds\n"
    "buffer_load_dwordx4 v222, s[12:15], s38 offen offset:2048 lds\n"
    "buffer_load_dwordx4 v223, s[12:15], s38 offen offset:3072 lds\n"
    "s_and_b32 s17, s17, 0xffff\n"
    "s_and_b32 s21, s21, 0xffff\n"
    "s_and_b32 s25, s25, 0xffff\n"
    "s_mov_b64 s[28:29], s[86:87]\n"
    "s_mov_b32 s30, 16777216\n"
    "s_mov_b32 s31, 0x20000\n"
    "s_mov_b64 s[32:33], s[84:85]\n"
    "s_mov_b32 s34, 2097152\n"
    "s_mov_b32 s35, 0x20000\n"
    "s_and_b32 s29, s29, 0xffff\n"
    "s_and_b32 s33, s33, 0xffff\n"
    : "={s[8:11]}"(sx), "={s[12:15]}"(swq), "={s[16:19]}"(sk), "={s[20:23]}"(sv), "={s[24:27]}"(swo), "={s[28:31]}"(sws), "={s[32:35]}"(sout), "+v"(tid)
    : "{s[64:79]}"(ka0), "{s[80:87]}"(ka1), "{s48}"(br), "{s36}"(sM0), "{s[56:57]}"(perm1), "{v[216:219]}"(voX), "{v[220:223]}"(voWq)
    : "s38", "s45", "a224", "a225", "scc", "memory");
  const unsigned l = tid & 63u, bp = w >> 1, ch = w & 1u, li = l & 15u, lg = l >> 4;
  u32x4 aRT, aTm, voV, voWo, vP4, vR4, vO4, vTmp, aR1, aK, voK;
  const unsigned q = li >> 2, p = l & 3u;
  const unsigned rpart = li * 128u + 16u * (lg ^ ((li >> 1) & 7u));
  aRT[0] = ldsb + bp * 8192u + rpart; aRT[1] = 0u;
  aRT[2] = ldsb + ch * 8192u + (8u * lg + q) * 128u + 16u * ((p >> 1) ^ (4u * (q >> 1)) ^ (2u * (lg & 1u))) + 8u * (p & 1u);
  aRT[3] = 0u;
  aR1[0] = ldsb + w * 4096u + rpart; aR1[1] = 0u; aR1[2] = 0u; aR1[3] = 0u;
  aK[0] = ldsb + w * 4096u + li * 128u + 16u * ((lg >> 1) ^ ((li >> 1) & 7u)) + 8u * (lg & 1u); aK[1] = 0u; aK[2] = 0u; aK[3] = 0u;
#pragma unroll
  for (unsigned i = 0; i < 4; ++i) {
    const unsigned swR = 16u * ((l & 7u) ^ ((4u * i + (l >> 4)) & 7u));
    const unsigned swT = 16u * ((l & 7u) ^ (4u * ((l >> 4) & 1u)) ^ (2u * (i & 1u)) ^ (4u * f0));
    voV[i] = ((batch_w * 32u + 8u * ksp) * 32u + 8u * i + (l >> 3)) * 256u + swR - 1024u * i;
    voWo[i] = (ksp * 1024u + 8u * (4u * (w & 1u) + i) + (l >> 3)) * 2048u + j2 * 256u + (w >> 1) * 128u + swT - 1024u * i;
    voK[i] = ((batch_w * 32u + head) * 32u + 8u * i + (l >> 3)) * 256u + ch * 128u + (swR ^ (64u * f0)) - 1024u * i;
  }
  aTm[0] = 0u; aTm[1] = 0u;
  aTm[2] = l * 16u;
  aTm[3] = ldsb + 32768u + w * 4096u + l * 16u;
  vP4[0] = WS_P + ((br * 16u + batch_w) * 32u + 8u * ksp) * 2048u + l * 16u;
  vP4[1] = WS_P + ((br * 16u + batch_w) * 32u + head) * 2048u + l * 16u;
  vP4[2] = WS_FLAGP + (((((l & 7u) * 4u + r) << 3) | g) * 4u + w) * 128u;
  vP4[3] = WS_FLAGP + (bid * 4u + w) * 128u;
  vR4[0] = WS_PART + (bid * 4u + w) * 3u * GRAN * 1024u + l * 16u;
  vR4[1] = 0u;
  vR4[2] = WS_FLAGR + (bid * 4u + w) * 128u;
  vR4[3] = WS_FLAGR + ((bid ^ (1u + (l % 3u))) * 4u + w) * 128u;
  vO4[0] = ((4u * r + 2u * bp + (ksp >> 1)) * 32u + li) * 4096u + br * 2048u + (j2 * 128u + ch * 64u + (ksp & 1u) * 32u + 4u * lg) * 2u;
  vO4[1] = WS_EP + (g * 4u + r) * 128u;
  vO4[2] = WS_ER + (br * 32u + idx) * 128u;
  vO4[3] = ldsb + 131072u + bp * 16384u + l * 16u;
#pragma unroll
  for (unsigned d = 1; d < 4; ++d) vTmp[d - 1] = WS_PART + (((bid ^ d) * 4u + w) * 3u + (d ^ 3u)) * GRAN * 1024u + l * 16u;
  vTmp[3] = WS_TS + (bid * 4u + w) * 256u;
  const unsigned sM0P = rfl(ldsb + w * 8192u);
  const unsigned scale = 0x3e0293eeu;
  u32x2 bumpP, bumpR; bumpP[0] = rfl((jh == 0u && w == 0u) ? 1u : 0u); bumpP[1] = 0u; bumpR[0] = rfl((ksp == 0u && w == 0u) ? 1u : 0u); bumpR[1] = 0u;
  asm volatile(
    "s_nop 4\n"
    "v_xor_b32 v193, 64, v192\n"
    "v_xor_b32 v7, 32, v6\n"
    "v_xor_b32 v8, 64, v6\n"
    "v_xor_b32 v9, 96, v6\n"
    "v_add_u32 v194, 0x10000, v192\n"
    "v_add_u32 v195, 0x10000, v193\n"
    "v_add_u32 v48, 0x10000, v6\n"
    "v_add_u32 v49, 0x10000, v7\n"
    "v_add_u32 v50, 0x10000, v8\n"
    "v_add_u32 v51, 0x10000, v9\n"
    "v_add_u32 v196, 0x20000, v192\n"
    "v_add_u32 v197, 0x20000, v193\n"
    "v_add_u32 v52, 0x20000, v6\n"
    "v_add_u32 v53, 0x20000, v7\n"
    "v_add_u32 v54, 0x20000, v8\n"
    "v_add_u32 v55, 0x20000, v9\n"
    "v_xor_b32 v198, 0x1000, v192\n"
    "v_xor_b32 v199, 0x1000, v193\n"
    "v_xor_b32 v200, 0x1000, v194\n"
    "v_xor_b32 v201, 0x1000, v195\n"
    "v_xor_b32 v202, 0x1000, v196\n"
    "v_xor_b32 v203, 0x1000, v197\n"
    "s_waitcnt vmcnt(8)\n"
    "s_barrier\n"
    "ds_read_b128 a[0:3], v192 offset:0\n"
    "ds_read_b64_tr_b16 a[16:17], v6 offset:16384\n"
    "ds_read_b64_tr_b16 a[18:19], v6 offset:16896\n"
    "ds_read_b128 a[4:7], v192 offset:2048\n"
    "ds_read_b64_tr_b16 a[20:21], v7 offset:16384\n"
    "ds_read_b64_tr_b16 a[22:23], v7 offset:16896\n"
    "ds_read_b128 a[8:11], v198 offset:0\n"
    "ds_read_b64_tr_b16 a[24:25], v8 offset:16384\n"
    "ds_read_b64_tr_b16 a[26:27], v8 offset:16896\n"
    "ds_read_b128 a[12:15], v198 offset:2048\n"
    "ds_read_b64_tr_b16 a[28:29], v9 offset:16384\n"
    "ds_read_b64_tr_b16 a[30:31], v9 offset:16896\n"
    "; ================ period 0 ================\n"
    "s_waitcnt lgkmcnt(3)\n"
    "v_mfma_f32_16x16x32_bf16 v[64:67], a[16:19], a[0:3], 0\n"
    "ds_read_b128 a[32:35], v193 offset:0\n"
    "v_mfma_f32_16x16x32_bf16 v[68:71], a[16:19], a[4:7], 0\n"
    "ds_read_b64_tr_b16 a[48:49], v6 offset:20480\n"
    "s_add_u32 m0, s36, 65536\n"
    "s_bfe_u32 s38, s56, 0x40008\n"
    "s_lshl_b32 s38, s38, 7\n"
    "buffer_load_dwordx4 v216, s[8:11], s38 offen lds\n"
    "v_xor_b32 v5, 64, v4\n"
    "v_mfma_f32_16x16x32_bf16 v[80:83], a[20:23], a[0:3], 0\n"
    "ds_read_b64_tr_b16 a[50:51], v6 offset:20992\n"
    "v_mfma_f32_16x16x32_bf16 v[84:87], a[20:23], a[4:7], 0\n"
    "ds_read_b128 a[36:39], v193 offset:2048\n"
    "buffer_load_dwordx4 v217, s[8:11], s38 offen offset:1024 lds\n"
    "buffer_load_dwordx4 v218, s[8:11], s38 offen offset:2048 lds\n"
    "v_add_u32 v44, 0x10000, v4\n"
    "v_mfma_f32_16x16x32_bf16 v[72:75], a[16:19], a[8:11], 0\n"
    "ds_read_b64_tr_b16 a[52:53], v7 offset:20480\n"
    "v_mfma_f32_16x16x32_bf16 v[88:91], a[20:23], a[8:11], 0\n"
    "ds_read_b64_tr_b16 a[54:55], v7 offset:20992\n"
    "buffer_load_dwordx4 v219, s[8:11], s38 offen offset:3072 lds\n"
    "v_add_u32 v45, 0x10000, v5\n"
    "v_mfma_f32_16x16x32_bf16 v[96:99], a[24:27], a[0:3], 0\n"
    "ds_read_b128 a[40:43], v199 offset:0\n"
    "v_mfma_f32_16x16x32_bf16 v[100:103], a[24:27], a[4:7], 0\n"
    "ds_read_b64_tr_b16 a[56:57], v8 offset:20480\n"
    "s_add_u32 m0, s36, 81920\n"
    "s_bfe_u32 s38, s56, 0x40008\n"
    "s_lshl_b32 s38, s38, 19\n"
    "buffer_load_dwordx4 v220, s[12:15], s38 offen lds\n"
    "buffer_load_dwordx4 v221, s[12:15], s38 offen offset:1024 lds\n"
    "v_add_u32 v46, 0x20000, v4\n"
    "v_mfma_f32_16x16x32_bf16 v[104:107], a[24:27], a[8:11], 0\n"
    "ds_read_b64_tr_b16 a[58:59], v8 offset:20992\n"
    "s_waitcnt lgkmcnt(11)\n"
    "v_mfma_f32_16x16x32_bf16 v[76:79], a[16:19], a[12:15], 0\n"
    "ds_read_b128 a[44:47], v199 offset:2048\n"
    "buffer_load_dwordx4 v222, s[12:15], s38 offen offset:2048 lds\n"
    "v_add_u32 v47, 0x20000, v5\n"
    "v_mfma_f32_16x16x32_bf16 v[92:95], a[20:23], a[12:15], 0\n"
    "ds_read_b64_tr_b16 a[60:61], v9 offset:20480\n"
    "v_mfma_f32_16x16x32_bf16 v[108:111], a[24:27], a[12:15], 0\n"
    "ds_read_b64_tr_b16 a[62:63], v9 offset:20992\n"
    "buffer_load_dwordx4 v223, s[12:15], s38 offen offset:3072 lds\n"
    "s_add_u32 m0, s36, 98304\n"
    "s_bfe_u32 s38, s56, 0x4000c\n"
    "s_lshl_b32 s38, s38, 7\n"
    "buffer_load_dwordx4 v216, s[8:11], s38 offen lds\n"
    "v_xor_b32 v205, 32, v204\n"
    "s_waitcnt lgkmcnt(12)\n"
    "v_mfma_f32_16x16x32_bf16 v[112:115], a[28:31], a[0:3], 0\n"
    "v_mfma_f32_16x16x32_bf16 v[116:119], a[28:31], a[4:7], 0\n"
    "buffer_load_dwordx4 v217, s[8:11], s38 offen offset:1024 lds\n"
    "v_xor_b32 v206, 64, v204\n"
    "v_mfma_f32_16x16x32_bf16 v[120:123], a[28:31], a[8:11], 0\n"
    "v_mfma_f32_16x16x32_bf16 v[124:127], a[28:31], a[12:15], 0\n"
    "buffer_load_dwordx4 v218, s[8:11], s38 offen offset:2048 lds\n"
    "buffer_load_dwordx4 v219, s[8:11], s38 offen offset:3072 lds\n"
    "v_xor_b32 v207, 96, v204\n"
    "s_waitcnt vmcnt(12)\n"
    "s_barrier\n"
    "s_waitcnt lgkmcnt(3)\n"
    "v_mfma_f32_16x16x32_bf16 v[64:67], a[48:51], a[32:35], v[64:67]\n"
    "ds_read_b128 a[0:3], v192 offset:32768\n"
    "v_mfma_f32_16x16x32_bf16 v[68:71], a[48:51], a[36:39], v[68:71]\n"
    "ds_read_b64_tr_b16 a[16:17], v6 offset:49152\n"
    "s_add_u32 m0, s36, 114688\n"
    "s_bfe_u32 s38, s56, 0x4000c\n"
    "s_lshl_b32 s38, s38, 19\n"
    "buffer_load_dwordx4 v220, s[12:15], s38 offen lds\n"
    "v_xor_b32 v43, 0x1000, v11\n"
    "v_mfma_f32_16x16x32_bf16 v[80:83], a[52:55], a[32:35], v[80:83]\n"
    "ds_read_b64_tr_b16 a[18:19], v6 offset:49664\n"
    "v_mfma_f32_16x16x32_bf16 v[84:87], a[52:55], a[36:39], v[84:87]\n"
    "ds_read_b128 a[4:7], v192 offset:34816\n"
    "buffer_load_dwordx4 v221, s[12:15], s38 offen offset:1024 lds\n"
    "buffer_load_dwordx4 v222, s[12:15], s38 offen offset:2048 lds\n"
    "v_add_u32 v42, 0xfffe0000, v31\n"
    "v_mfma_f32_16x16x32_bf16 v[72:75], a[48:51], a[40:43], v[72:75]\n"
    "ds_read_b64_tr_b16 a[20:21], v7 offset:49152\n"
    "v_mfma_f32_16x16x32_bf16 v[88:91], a[52:55], a[40:43], v[88:91]\n"
    "ds_read_b64_tr_b16 a[22:23], v7 offset:49664\n"
    "buffer_load_dwordx4 v223, s[12:15], s38 offen offset:3072 lds\n"
    "s_mov_b32 s46, 0x10000\n"
    "v_mfma_f32_16x16x32_bf16 v[96:99], a[56:59], a[32:35], v[96:99]\n"
    "ds_read_b128 a[8:11], v198 offset:32768\n"
    "v_mfma_f32_16x16x32_bf16 v[100:103], a[56:59], a[36:39], v[100:103]\n"
    "ds_read_b64_tr_b16 a[24:25], v8 offset:49152\n"
    "s_add_u32 m0, s36, 131072\n"
    "s_bfe_u32 s38, s56, 0x40010\n"
    "s_lshl_b32 s38, s38, 7\n"
    "buffer_load_dwordx4 v216, s[8:11], s38 offen lds\n"
    "buffer_load_dwordx4 v217, s[8:11], s38 offen offset:1024 lds\n"
    "v_mfma_f32_16x16x32_bf16 v[104:107], a[56:59], a[40:43], v[104:107]\n"
    "ds_read_b64_tr_b16 a[26:27], v8 offset:49664\n"
    "s_waitcnt lgkmcnt(11)\n"
    "v_mfma_f32_16x16x32_bf16 v[76:79], a[48:51], a[44:47], v[76:79]\n"
    "ds_read_b128 a[12:15], v198 offset:34816\n"
    "v_mfma_f32_16x16x32_bf16 v[92:95], a[52:55], a[44:47], v[92:95]\n"
    "ds_read_b64_tr_b16 a[28:29], v9 offset:49152\n"
    "v_mfma_f32_16x16x32_bf16 v[108:111], a[56:59], a[44:47], v[108:111]\n"
    "ds_read_b64_tr_b16 a[30:31], v9 offset:49664\n"
    "buffer_load_dwordx4 v218, s[8:11], s38 offen offset:2048 lds\n"
    "s_waitcnt lgkmcnt(12)\n"
    "v_mfma_f32_16x16x32_bf16 v[112:115], a[60:63], a[32:35], v[112:115]\n"
    "v_mfma_f32_16x16x32_bf16 v[116:119], a[60:63], a[36:39], v[116:119]\n"
    "v_mfma_f32_16x16x32_bf16 v[120:123], a[60:63], a[40:43], v[120:123]\n"
    "v_mfma_f32_16x16x32_bf16 v[124:127], a[60:63], a[44:47], v[124:127]\n"
    "buffer_load_dwordx4 v219, s[8:11], s38 offen offset:3072 lds\n"
    "; ================ period 1 ================\n"
    "s_waitcnt lgkmcnt(3)\n"
    "v_mfma_f32_16x16x32_bf16 v[64:67], a[16:19], a[0:3], v[64:67]\n"
    "ds_read_b128 a[32:35], v193 offset:32768\n"
    "v_mfma_f32_16x16x32_bf16 v[68:71], a[16:19], a[4:7], v[68:71]\n"
    "ds_read_b64_tr_b16 a[48:49], v6 offset:53248\n"
    "v_mfma_f32_16x16x32_bf16 v[80:83], a[20:23], a[0:3], v[80:83]\n"
    "ds_read_b64_tr_b16 a[50:51], v6 offset:53760\n"
    "v_mfma_f32_16x16x32_bf16 v[84:87], a[20:23], a[4:7], v[84:87]\n"
    "ds_read_b128 a[36:39], v193 offset:34816\n"
    "s_add_u32 m0, s36, 147456\n"
    "s_bfe_u32 s38, s56, 0x40010\n"
    "s_lshl_b32 s38, s38, 19\n"
    "buffer_load_dwordx4 v220, s[12:15], s38 offen lds\n"
    "v_mfma_f32_16x16x32_bf16 v[72:75], a[16:19], a[8:11], v[72:75]\n"
    "ds_read_b64_tr_b16 a[52:53], v7 offset:53248\n"
    "v_mfma_f32_16x16x32_bf16 v[88:91], a[20:23], a[8:11], v[88:91]\n"
    "ds_read_b64_tr_b16 a[54:55], v7 offset:53760\n"
    "v_mfma_f32_16x16x32_bf16 v[96:99], a[24:27], a[0:3], v[96:99]\n"
    "ds_read_b128 a[40:43], v199 offset:32768\n"
    "v_mfma_f32_16x16x32_bf16 v[100:103], a[24:27], a[4:7], v[100:103]\n"
    "ds_read_b64_tr_b16 a[56:57], v8 offset:53248\n"
    "buffer_load_dwordx4 v221, s[12:15], s38 offen offset:1024 lds\n"
    "v_mfma_f32_16x16x32_bf16 v[104:107], a[24:27], a[8:11], v[104:107]\n"
    "ds_read_b64_tr_b16 a[58:59], v8 offset:53760\n"
    "s_waitcnt lgkmcnt(11)\n"
    "v_mfma_f32_16x16x32_bf16 v[76:79], a[16:19], a[12:15], v[76:79]\n"
    "ds_read_b128 a[44:47], v199 offset:34816\n"
    "v_mfma_f32_16x16x32_bf16 v[92:95], a[20:23], a[12:15], v[92:95]\n"
    "ds_read_b64_tr_b16 a[60:61], v9 offset:53248\n"
    "v_mfma_f32_16x16x32_bf16 v[108:111], a[24:27], a[12:15], v[108:111]\n"
    "ds_read_b64_tr_b16 a[62:63], v9 offset:53760\n"
    "buffer_load_dwordx4 v222, s[12:15], s38 offen offset:2048 lds\n"
    "s_waitcnt lgkmcnt(12)\n"
    "v_mfma_f32_16x16x32_bf16 v[112:115], a[28:31], a[0:3], v[112:115]\n"
    "v_mfma_f32_16x16x32_bf16 v[116:119], a[28:31], a[4:7], v[116:119]\n"
    "v_mfma_f32_16x16x32_bf16 v[120:123], a[28:31], a[8:11], v[120:123]\n"
    "v_mfma_f32_16x16x32_bf16 v[124:127], a[28:31], a[12:15], v[124:127]\n"
    "buffer_load_dwordx4 v223, s[12:15], s38 offen offset:3072 lds\n"
    "s_waitcnt vmcnt(16)\n"
    "s_barrier\n"
    "s_waitcnt lgkmcnt(3)\n"
    "v_mfma_f32_16x16x32_bf16 v[64:67], a[48:51], a[32:35], v[64:67]\n"
    "ds_read_b128 a[0:3], v194 offset:0\n"
    "v_mfma_f32_16x16x32_bf16 v[68:71], a[48:51], a[36:39], v[68:71]\n"
    "ds_read_b64_tr_b16 a[16:17], v48 offset:16384\n"
    "buffer_load_dwordx2 v[32:33], v29, s[28:31], 0 offen sc1\n"
    "v_mfma_f32_16x16x32_bf16 v[80:83], a[52:55], a[32:35], v[80:83]\n"
    "ds_read_b64_tr_b16 a[18:19], v48 offset:16896\n"
    "v_mfma_f32_16x16x32_bf16 v[84:87], a[52:55], a[36:39], v[84:87]\n"
    "ds_read_b128 a[4:7], v194 offset:2048\n"
    "buffer_load_dwordx2 v[34:35], v30, s[28:31], 0 offen sc1\n"
    "v_mfma_f32_16x16x32_bf16 v[72:75], a[48:51], a[40:43], v[72:75]\n"
    "ds_read_b64_tr_b16 a[20:21], v49 offset:16384\n"
    "v_mfma_f32_16x16x32_bf16 v[88:91], a[52:55], a[40:43], v[88:91]\n"
    "ds_read_b64_tr_b16 a[22:23], v49 offset:16896\n"
    "v_mfma_f32_16x16x32_bf16 v[96:99], a[56:59], a[32:35], v[96:99]\n"
    "ds_read_b128 a[8:11], v200 offset:0\n"
    "v_mfma_f32_16x16x32_bf16 v[100:103], a[56:59], a[36:39], v[100:103]\n"
    "ds_read_b64_tr_b16 a[24:25], v50 offset:16384\n"
    "s_add_u32 m0, s36, 0\n"
    "s_bfe_u32 s38, s56, 0x40014\n"
    "s_lshl_b32 s38, s38, 7\n"
    "buffer_load_dwordx4 v216, s[8:11], s38 offen lds\n"
    "v_mfma_f32_16x16x32_bf16 v[104:107], a[56:59], a[40:43], v[104:107]\n"
    "ds_read_b64_tr_b16 a[26:27], v50 offset:16896\n"
    "s_waitcnt lgkmcnt(11)\n"
    "v_mfma_f32_16x16x32_bf16 v[76:79], a[48:51], a[44:47], v[76:79]\n"
    "ds_read_b128 a[12:15], v200 offset:2048\n"
    "v_mfma_f32_16x16x32_bf16 v[92:95], a[52:55], a[44:47], v[92:95]\n"
    "ds_read_b64_tr_b16 a[28:29], v51 offset:16384\n"
    "v_mfma_f32_16x16x32_bf16 v[108:111], a[56:59], a[44:47], v[108:111]\n"
    "ds_read_b64_tr_b16 a[30:31], v51 offset:16896\n"
    "buffer_load_dwordx4 v217, s[8:11], s38 offen offset:1024 lds\n"
    "s_waitcnt lgkmcnt(12)\n"
    "v_mfma_f32_16x16x32_bf16 v[112:115], a[60:63], a[32:35], v[112:115]\n"
    "v_mfma_f32_16x16x32_bf16 v[116:119], a[60:63], a[36:39], v[116:119]\n"
    "v_mfma_f32_16x16x32_bf16 v[120:123], a[60:63], a[40:43], v[120:123]\n"
    "v_mfma_f32_16x16x32_bf16 v[124:127], a[60:63], a[44:47], v[124:127]\n"
    "buffer_load_dwordx4 v218, s[8:11], s38 offen offset:2048 lds\n"
    "; ================ period 2 ================\n"
    "s_waitcnt lgkmcnt(3)\n"
    "v_mfma_f32_16x16x32_bf16 v[64:67], a[16:19], a[0:3], v[64:67]\n"
    "ds_read_b128 a[32:35], v195 offset:0\n"
    "v_mfma_f32_16x16x32_bf16 v[68:71], a[16:19], a[4:7], v[68:71]\n"
    "ds_read_b64_tr_b16 a[48:49], v48 offset:20480\n"
    "v_mfma_f32_16x16x32_bf16 v[80:83], a[20:23], a[0:3], v[80:83]\n"
    "ds_read_b64_tr_b16 a[50:51], v48 offset:20992\n"
    "v_mfma_f32_16x16x32_bf16 v[84:87], a[20:23], a[4:7], v[84:87]\n"
    "ds_read_b128 a[36:39], v195 offset:2048\n"
    "buffer_load_dwordx4 v219, s[8:11], s38 offen offset:3072 lds\n"
    "v_mfma_f32_16x16x32_bf16 v[72:75], a[16:19], a[8:11], v[72:75]\n"
    "ds_read_b64_tr_b16 a[52:53], v49 offset:20480\n"
    "v_mfma_f32_16x16x32_bf16 v[88:91], a[20:23], a[8:11], v[88:91]\n"
    "ds_read_b64_tr_b16 a[54:55], v49 offset:20992\n"
    "v_mfma_f32_16x16x32_bf16 v[96:99], a[24:27], a[0:3], v[96:99]\n"
    "ds_read_b128 a[40:43], v201 offset:0\n"
    "v_mfma_f32_16x16x32_bf16 v[100:103], a[24:27], a[4:7], v[100:103]\n"
    "ds_read_b64_tr_b16 a[56:57], v50 offset:20480\n"
    "s_add_u32 m0, s36, 16384\n"
    "s_bfe_u32 s38, s56, 0x40014\n"
    "s_lshl_b32 s38, s38, 19\n"
    "buffer_load_dwordx4 v220, s[12:15], s38 offen lds\n"
    "v_mfma_f32_16x16x32_bf16 v[104:107], a[24:27], a[8:11], v[104:107]\n"
    "ds_read_b64_tr_b16 a[58:59], v50 offset:20992\n"
    "s_waitcnt lgkmcnt(11)\n"
    "v_mfma_f32_16x16x32_bf16 v[76:79], a[16:19], a[12:15], v[76:79]\n"
    "ds_read_b128 a[44:47], v201 offset:2048\n"
    "v_mfma_f32_16x16x32_bf16 v[92:95], a[20:23], a[12:15], v[92:95]\n"
    "ds_read_b64_tr_b16 a[60:61], v51 offset:20480\n"
    "v_mfma_f32_16x16x32_bf16 v[108:111], a[24:27], a[12:15], v[108:111]\n"
    "ds_read_b64_tr_b16 a[62:63], v51 offset:20992\n"
    "buffer_load_dwordx4 v221, s[12:15], s38 offen offset:1024 lds\n"
    "s_waitcnt lgkmcnt(12)\n"
    "v_mfma_f32_16x16x32_bf16 v[112:115], a[28:31], a[0:3], v[112:115]\n"
    "v_mfma_f32_16x16x32_bf16 v[116:119], a[28:31], a[4:7], v[116:119]\n"
    "v_mfma_f32_16x16x32_bf16 v[120:123], a[28:31], a[8:11], v[120:123]\n"
    "v_mfma_f32_16x16x32_bf16 v[124:127], a[28:31], a[12:15], v[124:127]\n"
    "buffer_load_dwordx4 v222, s[12:15], s38 offen offset:2048 lds\n"
    "s_waitcnt vmcnt(17)\n"
    "s_barrier\n"
    "s_waitcnt lgkmcnt(3)\n"
    "v_mfma_f32_16x16x32_bf16 v[64:67], a[48:51], a[32:35], v[64:67]\n"
    "ds_read_b128 a[0:3], v194 offset:32768\n"
    "v_mfma_f32_16x16x32_bf16 v[68:71], a[48:51], a[36:39], v[68:71]\n"
    "ds_read_b64_tr_b16 a[16:17], v48 offset:49152\n"
    "buffer_load_dwordx4 v223, s[12:15], s38 offen offset:3072 lds\n"
    "v_mfma_f32_16x16x32_bf16 v[80:83], a[52:55], a[32:35], v[80:83]\n"
    "ds_read_b64_tr_b16 a[18:19], v48 offset:49664\n"
    "v_mfma_f32_16x16x32_bf16 v[84:87], a[52:55], a[36:39], v[84:87]\n"
    "ds_read_b128 a[4:7], v194 offset:34816\n"
    "s_add_u32 m0, s36, 32768\n"
    "s_bfe_u32 s38, s56, 0x40018\n"
    "s_lshl_b32 s38, s38, 7\n"
    "buffer_load_dwordx4 v216, s[8:11], s38 offen lds\n"
    "v_mfma_f32_16x16x32_bf16 v[72:75], a[48:51], a[40:43], v[72:75]\n"
    "ds_read_b64_tr_b16 a[20:21], v49 offset:49152\n"
    "v_mfma_f32_16x16x32_bf16 v[88:91], a[52:55], a[40:43], v[88:91]\n"
    "ds_read_b64_tr_b16 a[22:23], v49 offset:49664\n"
    "v_mfma_f32_16x16x32_bf16 v[96:99], a[56:59], a[32:35], v[96:99]\n"
    "ds_read_b128 a[8:11], v200 offset:32768\n"
    "v_mfma_f32_16x16x32_bf16 v[100:103], a[56:59], a[36:39], v[100:103]\n"
    "ds_read_b64_tr_b16 a[24:25], v50 offset:49152\n"
    "buffer_load_dwordx4 v217, s[8:11], s38 offen offset:1024 lds\n"
    "v_mfma_f32_16x16x32_bf16 v[104:107], a[56:59], a[40:43], v[104:107]\n"
    "ds_read_b64_tr_b16 a[26:27], v50 offset:49664\n"
    "s_waitcnt lgkmcnt(11)\n"
    "v_mfma_f32_16x16x32_bf16 v[76:79], a[48:51], a[44:47], v[76:79]\n"
    "ds_read_b128 a[12:15], v200 offset:34816\n"
    "v_mfma_f32_16x16x32_bf16 v[92:95], a[52:55], a[44:47], v[92:95]\n"
    "ds_read_b64_tr_b16 a[28:29], v51 offset:49152\n"
    "v_mfma_f32_16x16x32_bf16 v[108:111], a[56:59], a[44:47], v[108:111]\n"
    "ds_read_b64_tr_b16 a[30:31], v51 offset:49664\n"
    "buffer_load_dwordx4 v218, s[8:11], s38 offen offset:2048 lds\n"
    "s_waitcnt lgkmcnt(12)\n"
    "v_mfma_f32_16x16x32_bf16 v[112:115], a[60:63], a[32:35], v[112:115]\n"
    "v_mfma_f32_16x16x32_bf16 v[116:119], a[60:63], a[36:39], v[116:119]\n"
    "v_mfma_f32_16x16x32_bf16 v[120:123], a[60:63], a[40:43], v[120:123]\n"
    "v_mfma_f32_16x16x32_bf16 v[124:127], a[60:63], a[44:47], v[124:127]\n"
    "buffer_load_dwordx4 v219, s[8:11], s38 offen offset:3072 lds\n"
    "; ================ period 3 ================\n"
    "s_waitcnt lgkmcnt(3)\n"
    "v_mfma_f32_16x16x32_bf16 v[64:67], a[16:19], a[0:3], v[64:67]\n"
    "ds_read_b128 a[32:35], v195 offset:32768\n"
    "v_mfma_f32_16x16x32_bf16 v[68:71], a[16:19], a[4:7], v[68:71]\n"
    "ds_read_b64_tr_b16 a[48:49], v48 offset:53248\n"
    "v_mfma_f32_16x16x32_bf16 v[80:83], a[20:23], a[0:3], v[80:83]\n"
    "ds_read_b64_tr_b16 a[50:51], v48 offset:53760\n"
    "v_mfma_f32_16x16x32_bf16 v[84:87], a[20:23], a[4:7], v[84:87]\n"
    "ds_read_b128 a[36:39], v195 offset:34816\n"
    "s_add_u32 m0, s36, 49152\n"
    "s_bfe_u32 s38, s56, 0x40018\n"
    "s_lshl_b32 s38, s38, 19\n"
    "buffer_load_dwordx4 v220, s[12:15], s38 offen lds\n"
    "v_mfma_f32_16x16x32_bf16 v[72:75], a[16:19], a[8:11], v[72:75]\n"
    "ds_read_b64_tr_b16 a[52:53], v49 offset:53248\n"
    "v_mfma_f32_16x16x32_bf16 v[88:91], a[20:23], a[8:11], v[88:91]\n"
    "ds_read_b64_tr_b16 a[54:55], v49 offset:53760\n"
    "v_mfma_f32_16x16x32_bf16 v[96:99], a[24:27], a[0:3], v[96:99]\n"
    "ds_read_b128 a[40:43], v201 offset:32768\n"
    "v_mfma_f32_16x16x32_bf16 v[100:103], a[24:27], a[4:7], v[100:103]\n"
    "ds_read_b64_tr_b16 a[56:57], v50 offset:53248\n"
    "buffer_load_dwordx4 v221, s[12:15], s38 offen offset:1024 lds\n"
    "v_mfma_f32_16x16x32_bf16 v[104:107], a[24:27], a[8:11], v[104:107]\n"
    "ds_read_b64_tr_b16 a[58:59], v50 offset:53760\n"
    "s_waitcnt lgkmcnt(11)\n"
    "v_mfma_f32_16x16x32_bf16 v[76:79], a[16:19], a[12:15], v[76:79]\n"
    "ds_read_b128 a[44:47], v201 offset:34816\n"
    "v_mfma_f32_16x16x32_bf16 v[92:95], a[20:23], a[12:15], v[92:95]\n"
    "ds_read_b64_tr_b16 a[60:61], v51 offset:53248\n"
    "v_mfma_f32_16x16x32_bf16 v[108:111], a[24:27], a[12:15], v[108:111]\n"
    "ds_read_b64_tr_b16 a[62:63], v51 offset:53760\n"
    "buffer_load_dwordx4 v222, s[12:15], s38 offen offset:2048 lds\n"
    "s_waitcnt lgkmcnt(12)\n"
    "v_mfma_f32_16x16x32_bf16 v[112:115], a[28:31], a[0:3], v[112:115]\n"
    "v_mfma_f32_16x16x32_bf16 v[116:119], a[28:31], a[4:7], v[116:119]\n"
    "v_mfma_f32_16x16x32_bf16 v[120:123], a[28:31], a[8:11], v[120:123]\n"
    "v_mfma_f32_16x16x32_bf16 v[124:127], a[28:31], a[12:15], v[124:127]\n"
    "buffer_load_dwordx4 v223, s[12:15], s38 offen offset:3072 lds\n"
    "s_waitcnt vmcnt(18)\n"
    "s_barrier\n"
    "s_waitcnt lgkmcnt(3)\n"
    "v_mfma_f32_16x16x32_bf16 v[64:67], a[48:51], a[32:35], v[64:67]\n"
    "ds_read_b128 a[0:3], v196 offset:0\n"
    "v_mfma_f32_16x16x32_bf16 v[68:71], a[48:51], a[36:39], v[68:71]\n"
    "ds_read_b64_tr_b16 a[16:17], v52 offset:16384\n"
    "v_mfma_f32_16x16x32_bf16 v[80:83], a[52:55], a[32:35], v[80:83]\n"
    "ds_read_b64_tr_b16 a[18:19], v52 offset:16896\n"
    "v_mfma_f32_16x16x32_bf16 v[84:87], a[52:55], a[36:39], v[84:87]\n"
    "ds_read_b128 a[4:7], v196 offset:2048\n"
    "s_add_u32 m0, s36, 65536\n"
    "s_bfe_u32 s38, s56, 0x4001c\n"
    "s_lshl_b32 s38, s38, 7\n"
    "buffer_load_dwordx4 v216, s[8:11], s38 offen lds\n"
    "v_mfma_f32_16x16x32_bf16 v[72:75], a[48:51], a[40:43], v[72:75]\n"
    "ds_read_b64_tr_b16 a[20:21], v53 offset:16384\n"
    "v_mfma_f32_16x16x32_bf16 v[88:91], a[52:55], a[40:43], v[88:91]\n"
    "ds_read_b64_tr_b16 a[22:23], v53 offset:16896\n"
    "v_mfma_f32_16x16x32_bf16 v[96:99], a[56:59], a[32:35], v[96:99]\n"
    "ds_read_b128 a[8:11], v202 offset:0\n"
    "v_mfma_f32_16x16x32_bf16 v[100:103], a[56:59], a[36:39], v[100:103]\n"
    "ds_read_b64_tr_b16 a[24:25], v54 offset:16384\n"
    "buffer_load_dwordx4 v217, s[8:11], s38 offen offset:1024 lds\n"
    "v_mfma_f32_16x16x32_bf16 v[104:107], a[56:59], a[40:43], v[104:107]\n"
    "ds_read_b64_tr_b16 a[26:27], v54 offset:16896\n"
    "s_waitcnt lgkmcnt(11)\n"
    "v_mfma_f32_16x16x32_bf16 v[76:79], a[48:51], a[44:47], v[76:79]\n"
    "ds_read_b128 a[12:15], v202 offset:2048\n"
    "v_mfma_f32_16x16x32_bf16 v[92:95], a[52:55], a[44:47], v[92:95]\n"
    "ds_read_b64_tr_b16 a[28:29], v55 offset:16384\n"
    "v_mfma_f32_16x16x32_bf16 v[108:111], a[56:59], a[44:47], v[108:111]\n"
    "ds_read_b64_tr_b16 a[30:31], v55 offset:16896\n"
    "buffer_load_dwordx4 v218, s[8:11], s38 offen offset:2048 lds\n"
    "s_waitcnt lgkmcnt(12)\n"
    "v_mfma_f32_16x16x32_bf16 v[112:115], a[60:63], a[32:35], v[112:115]\n"
    "v_mfma_f32_16x16x32_bf16 v[116:119], a[60:63], a[36:39], v[116:119]\n"
    "v_mfma_f32_16x16x32_bf16 v[120:123], a[60:63], a[40:43], v[120:123]\n"
    "v_mfma_f32_16x16x32_bf16 v[124:127], a[60:63], a[44:47], v[124:127]\n"
    "buffer_load_dwordx4 v219, s[8:11], s38 offen offset:3072 lds\n"
    "; ================ period 4 ================\n"
    "s_waitcnt lgkmcnt(3)\n"
    "v_mfma_f32_16x16x32_bf16 v[64:67], a[16:19], a[0:3], v[64:67]\n"
    "ds_read_b128 a[32:35], v197 offset:0\n"
    "v_mfma_f32_16x16x32_bf16 v[68:71], a[16:19], a[4:7], v[68:71]\n"
    "ds_read_b64_tr_b16 a[48:49], v52 offset:20480\n"
    "v_mfma_f32_16x16x32_bf16 v[80:83], a[20:23], a[0:3], v[80:83]\n"
    "ds_read_b64_tr_b16 a[50:51], v52 offset:20992\n"
    "v_mfma_f32_16x16x32_bf16 v[84:87], a[20:23], a[4:7], v[84:87]\n"
    "ds_read_b128 a[36:39], v197 offset:2048\n"
    "s_add_u32 m0, s36, 81920\n"
    "s_bfe_u32 s38, s56, 0x4001c\n"
    "s_lshl_b32 s38, s38, 19\n"
    "buffer_load_dwordx4 v220, s[12:15], s38 offen lds\n"
    "v_mfma_f32_16x16x32_bf16 v[72:75], a[16:19], a[8:11], v[72:75]\n"
    "ds_read_b64_tr_b16 a[52:53], v53 offset:20480\n"
    "v_mfma_f32_16x16x32_bf16 v[88:91], a[20:23], a[8:11], v[88:91]\n"
    "ds_read_b64_tr_b16 a[54:55], v53 offset:20992\n"
    "v_mfma_f32_16x16x32_bf16 v[96:99], a[24:27], a[0:3], v[96:99]\n"
    "ds_read_b128 a[40:43], v203 offset:0\n"
    "v_mfma_f32_16x16x32_bf16 v[100:103], a[24:27], a[4:7], v[100:103]\n"
    "ds_read_b64_tr_b16 a[56:57], v54 offset:20480\n"
    "buffer_load_dwordx4 v221, s[12:15], s38 offen offset:1024 lds\n"
    "v_mfma_f32_16x16x32_bf16 v[104:107], a[24:27], a[8:11], v[104:107]\n"
    "ds_read_b64_tr_b16 a[58:59], v54 offset:20992\n"
    "s_waitcnt lgkmcnt(11)\n"
    "v_mfma_f32_16x16x32_bf16 v[76:79], a[16:19], a[12:15], v[76:79]\n"
    "ds_read_b128 a[44:47], v203 offset:2048\n"
    "v_mfma_f32_16x16x32_bf16 v[92:95], a[20:23], a[12:15], v[92:95]\n"
    "ds_read_b64_tr_b16 a[60:61], v55 offset:20480\n"
    "v_mfma_f32_16x16x32_bf16 v[108:111], a[24:27], a[12:15], v[108:111]\n"
    "ds_read_b64_tr_b16 a[62:63], v55 offset:20992\n"
    "buffer_load_dwordx4 v222, s[12:15], s38 offen offset:2048 lds\n"
    "s_waitcnt lgkmcnt(12)\n"
    "v_mfma_f32_16x16x32_bf16 v[112:115], a[28:31], a[0:3], v[112:115]\n"
    "v_mfma_f32_16x16x32_bf16 v[116:119], a[28:31], a[4:7], v[116:119]\n"
    "v_mfma_f32_16x16x32_bf16 v[120:123], a[28:31], a[8:11], v[120:123]\n"
    "v_mfma_f32_16x16x32_bf16 v[124:127], a[28:31], a[12:15], v[124:127]\n"
    "buffer_load_dwordx4 v223, s[12:15], s38 offen offset:3072 lds\n"
    "s_waitcnt vmcnt(16)\n"
    "s_barrier\n"
    "s_waitcnt lgkmcnt(3)\n"
    "v_mfma_f32_16x16x32_bf16 v[64:67], a[48:51], a[32:35], v[64:67]\n"
    "ds_read_b128 a[0:3], v192 offset:0\n"
    "v_mfma_f32_16x16x32_bf16 v[68:71], a[48:51], a[36:39], v[68:71]\n"
    "ds_read_b64_tr_b16 a[16:17], v6 offset:16384\n"
    "v_mfma_f32_16x16x32_bf16 v[80:83], a[52:55], a[32:35], v[80:83]\n"
    "ds_read_b64_tr_b16 a[18:19], v6 offset:16896\n"
    "v_mfma_f32_16x16x32_bf16 v[84:87], a[52:55], a[36:39], v[84:87]\n"
    "ds_read_b128 a[4:7], v192 offset:2048\n"
    "s_add_u32 m0, s36, 98304\n"
    "s_bfe_u32 s38, s57, 0x40000\n"
    "s_lshl_b32 s38, s38, 7\n"
    "buffer_load_dwordx4 v216, s[8:11], s38 offen lds\n"
    "v_mfma_f32_16x16x32_bf16 v[72:75], a[48:51], a[40:43], v[72:75]\n"
    "ds_read_b64_tr_b16 a[20:21], v7 offset:16384\n"
    "v_mfma_f32_16x16x32_bf16 v[88:91], a[52:55], a[40:43], v[88:91]\n"
    "ds_read_b64_tr_b16 a[22:23], v7 offset:16896\n"
    "v_mfma_f32_16x16x32_bf16 v[96:99], a[56:59], a[32:35], v[96:99]\n"
    "ds_read_b128 a[8:11], v198 offset:0\n"
    "v_mfma_f32_16x16x32_bf16 v[100:103], a[56:59], a[36:39], v[100:103]\n"
    "ds_read_b64_tr_b16 a[24:25], v8 offset:16384\n"
    "buffer_load_dwordx4 v217, s[8:11], s38 offen offset:1024 lds\n"
    "v_mfma_f32_16x16x32_bf16 v[104:107], a[56:59], a[40:43], v[104:107]\n"
    "ds_read_b64_tr_b16 a[26:27], v8 offset:16896\n"
    "s_waitcnt lgkmcnt(11)\n"
    "v_mfma_f32_16x16x32_bf16 v[76:79], a[48:51], a[44:47], v[76:79]\n"
    "ds_read_b128 a[12:15], v198 offset:2048\n"
    "v_mfma_f32_16x16x32_bf16 v[92:95], a[52:55], a[44:47], v[92:95]\n"
    "ds_read_b64_tr_b16 a[28:29], v9 offset:16384\n"
    "v_mfma_f32_16x16x32_bf16 v[108:111], a[56:59], a[44:47], v[108:111]\n"
    "ds_read_b64_tr_b16 a[30:31], v9 offset:16896\n"
    "buffer_load_dwordx4 v218, s[8:11], s38 offen offset:2048 lds\n"
    "s_waitcnt lgkmcnt(12)\n"
    "v_mfma_f32_16x16x32_bf16 v[112:115], a[60:63], a[32:35], v[112:115]\n"
    "v_mfma_f32_16x16x32_bf16 v[116:119], a[60:63], a[36:39], v[116:119]\n"
    "v_mfma_f32_16x16x32_bf16 v[120:123], a[60:63], a[40:43], v[120:123]\n"
    "v_mfma_f32_16x16x32_bf16 v[124:127], a[60:63], a[44:47], v[124:127]\n"
    "buffer_load_dwordx4 v219, s[8:11], s38 offen offset:3072 lds\n"
    "; ================ period 5 ================\n"
    "s_waitcnt lgkmcnt(3)\n"
    "v_mfma_f32_16x16x32_bf16 v[64:67], a[16:19], a[0:3], v[64:67]\n"
    "ds_read_b128 a[32:35], v193 offset:0\n"
    "v_mfma_f32_16x16x32_bf16 v[68:71], a[16:19], a[4:7], v[68:71]\n"
    "ds_read_b64_tr_b16 a[48:49], v6 offset:20480\n"
    "v_mfma_f32_16x16x32_bf16 v[80:83], a[20:23], a[0:3], v[80:83]\n"
    "ds_read_b64_tr_b16 a[50:51], v6 offset:20992\n"
    "v_mfma_f32_16x16x32_bf16 v[84:87], a[20:23], a[4:7], v[84:87]\n"
    "ds_read_b128 a[36:39], v193 offset:2048\n"
    "s_add_u32 m0, s36, 114688\n"
    "s_bfe_u32 s38, s57, 0x40000\n"
    "s_lshl_b32 s38, s38, 19\n"
    "buffer_load_dwordx4 v220, s[12:15], s38 offen lds\n"
    "v_mfma_f32_16x16x32_bf16 v[72:75], a[16:19], a[8:11], v[72:75]\n"
    "ds_read_b64_tr_b16 a[52:53], v7 offset:20480\n"
    "v_mfma_f32_16x16x32_bf16 v[88:91], a[20:23], a[8:11], v[88:91]\n"
    "ds_read_b64_tr_b16 a[54:55], v7 offset:20992\n"
    "v_mfma_f32_16x16x32_bf16 v[96:99], a[24:27], a[0:3], v[96:99]\n"
    "ds_read_b128 a[40:43], v199 offset:0\n"
    "v_mfma_f32_16x16x32_bf16 v[100:103], a[24:27], a[4:7], v[100:103]\n"
    "ds_read_b64_tr_b16 a[56:57], v8 offset:20480\n"
    "buffer_load_dwordx4 v221, s[12:15], s38 offen offset:1024 lds\n"
    "v_mfma_f32_16x16x32_bf16 v[104:107], a[24:27], a[8:11], v[104:107]\n"
    "ds_read_b64_tr_b16 a[58:59], v8 offset:20992\n"
    "s_waitcnt lgkmcnt(11)\n"
    "v_mfma_f32_16x16x32_bf16 v[76:79], a[16:19], a[12:15], v[76:79]\n"
    "ds_read_b128 a[44:47], v199 offset:2048\n"
    "v_mfma_f32_16x16x32_bf16 v[92:95], a[20:23], a[12:15], v[92:95]\n"
    "ds_read_b64_tr_b16 a[60:61], v9 offset:20480\n"
    "v_mfma_f32_16x16x32_bf16 v[108:111], a[24:27], a[12:15], v[108:111]\n"
    "ds_read_b64_tr_b16 a[62:63], v9 offset:20992\n"
    "buffer_load_dwordx4 v222, s[12:15], s38 offen offset:2048 lds\n"
    "s_waitcnt lgkmcnt(12)\n"
    "v_mfma_f32_16x16x32_bf16 v[112:115], a[28:31], a[0:3], v[112:115]\n"
    "v_mfma_f32_16x16x32_bf16 v[116:119], a[28:31], a[4:7], v[116:119]\n"
    "v_mfma_f32_16x16x32_bf16 v[120:123], a[28:31], a[8:11], v[120:123]\n"
    "v_mfma_f32_16x16x32_bf16 v[124:127], a[28:31], a[12:15], v[124:127]\n"
    "buffer_load_dwordx4 v223, s[12:15], s38 offen offset:3072 lds\n"
    "s_waitcnt vmcnt(16)\n"
    "s_barrier\n"
    "s_waitcnt lgkmcnt(3)\n"
    "v_mfma_f32_16x16x32_bf16 v[64:67], a[48:51], a[32:35], v[64:67]\n"
    "ds_read_b128 a[0:3], v192 offset:32768\n"
    "v_mfma_f32_16x16x32_bf16 v[68:71], a[48:51], a[36:39], v[68:71]\n"
    "ds_read_b64_tr_b16 a[16:17], v6 offset:49152\n"
    "v_mfma_f32_16x16x32_bf16 v[80:83], a[52:55], a[32:35], v[80:83]\n"
    "ds_read_b64_tr_b16 a[18:19], v6 offset:49664\n"
    "v_mfma_f32_16x16x32_bf16 v[84:87], a[52:55], a[36:39], v[84:87]\n"
    "ds_read_b128 a[4:7], v192 offset:34816\n"
    "s_add_u32 m0, s36, 131072\n"
    "s_bfe_u32 s38, s57, 0x40004\n"
    "s_lshl_b32 s38, s38, 7\n"
    "buffer_load_dwordx4 v216, s[8:11], s38 offen lds\n"
    "v_mfma_f32_16x16x32_bf16 v[72:75], a[48:51], a[40:43], v[72:75]\n"
    "ds_read_b64_tr_b16 a[20:21], v7 offset:49152\n"
    "v_mfma_f32_16x16x32_bf16 v[88:91], a[52:55], a[40:43], v[88:91]\n"
    "ds_read_b64_tr_b16 a[22:23], v7 offset:49664\n"
    "v_mfma_f32_16x16x32_bf16 v[96:99], a[56:59], a[32:35], v[96:99]\n"
    "ds_read_b128 a[8:11], v198 offset:32768\n"
    "v_mfma_f32_16x16x32_bf16 v[100:103], a[56:59], a[36:39], v[100:103]\n"
    "ds_read_b64_tr_b16 a[24:25], v8 offset:49152\n"
    "buffer_load_dwordx4 v217, s[8:11], s38 offen offset:1024 lds\n"
    "v_mfma_f32_16x16x32_bf16 v[104:107], a[56:59], a[40:43], v[104:107]\n"
    "ds_read_b64_tr_b16 a[26:27], v8 offset:49664\n"
    "s_waitcnt lgkmcnt(11)\n"
    "v_mfma_f32_16x16x32_bf16 v[76:79], a[48:51], a[44:47], v[76:79]\n"
    "ds_read_b128 a[12:15], v198 offset:34816\n"
    "v_mfma_f32_16x16x32_bf16 v[92:95], a[52:55], a[44:47], v[92:95]\n"
    "ds_read_b64_tr_b16 a[28:29], v9 offset:49152\n"
    "v_mfma_f32_16x16x32_bf16 v[108:111], a[56:59], a[44:47], v[108:111]\n"
    "ds_read_b64_tr_b16 a[30:31], v9 offset:49664\n"
    "buffer_load_dwordx4 v218, s[8:11], s38 offen offset:2048 lds\n"
    "s_waitcnt lgkmcnt(12)\n"
    "v_mfma_f32_16x16x32_bf16 v[112:115], a[60:63], a[32:35], v[112:115]\n"
    "v_mfma_f32_16x16x32_bf16 v[116:119], a[60:63], a[36:39], v[116:119]\n"
    "v_mfma_f32_16x16x32_bf16 v[120:123], a[60:63], a[40:43], v[120:123]\n"
    "v_mfma_f32_16x16x32_bf16 v[124:127], a[60:63], a[44:47], v[124:127]\n"
    "buffer_load_dwordx4 v219, s[8:11], s38 offen offset:3072 lds\n"
    "; ================ period 6 ================\n"
    "s_waitcnt lgkmcnt(3)\n"
    "v_mfma_f32_16x16x32_bf16 v[64:67], a[16:19], a[0:3], v[64:67]\n"
    "ds_read_b128 a[32:35], v193 offset:32768\n"
    "v_mfma_f32_16x16x32_bf16 v[68:71], a[16:19], a[4:7], v[68:71]\n"
    "ds_read_b64_tr_b16 a[48:49], v6 offset:53248\n"
    "v_mfma_f32_16x16x32_bf16 v[80:83], a[20:23], a[0:3], v[80:83]\n"
    "ds_read_b64_tr_b16 a[50:51], v6 offset:53760\n"
    "v_mfma_f32_16x16x32_bf16 v[84:87], a[20:23], a[4:7], v[84:87]\n"
    "ds_read_b128 a[36:39], v193 offset:34816\n"
    "s_add_u32 m0, s36, 147456\n"
    "s_bfe_u32 s38, s57, 0x40004\n"
    "s_lshl_b32 s38, s38, 19\n"
    "buffer_load_dwordx4 v220, s[12:15], s38 offen lds\n"
    "v_mfma_f32_16x16x32_bf16 v[72:75], a[16:19], a[8:11], v[72:75]\n"
    "ds_read_b64_tr_b16 a[52:53], v7 offset:53248\n"
    "v_mfma_f32_16x16x32_bf16 v[88:91], a[20:23], a[8:11], v[88:91]\n"
    "ds_read_b64_tr_b16 a[54:55], v7 offset:53760\n"
    "v_mfma_f32_16x16x32_bf16 v[96:99], a[24:27], a[0:3], v[96:99]\n"
    "ds_read_b128 a[40:43], v199 offset:32768\n"
    "v_mfma_f32_16x16x32_bf16 v[100:103], a[24:27], a[4:7], v[100:103]\n"
    "ds_read_b64_tr_b16 a[56:57], v8 offset:53248\n"
    "buffer_load_dwordx4 v221, s[12:15], s38 offen offset:1024 lds\n"
    "v_mfma_f32_16x16x32_bf16 v[104:107], a[24:27], a[8:11], v[104:107]\n"
    "ds_read_b64_tr_b16 a[58:59], v8 offset:53760\n"
    "s_waitcnt lgkmcnt(11)\n"
    "v_mfma_f32_16x16x32_bf16 v[76:79], a[16:19], a[12:15], v[76:79]\n"
    "ds_read_b128 a[44:47], v199 offset:34816\n"
    "v_mfma_f32_16x16x32_bf16 v[92:95], a[20:23], a[12:15], v[92:95]\n"
    "ds_read_b64_tr_b16 a[60:61], v9 offset:53248\n"
    "v_mfma_f32_16x16x32_bf16 v[108:111], a[24:27], a[12:15], v[108:111]\n"
    "ds_read_b64_tr_b16 a[62:63], v9 offset:53760\n"
    "buffer_load_dwordx4 v222, s[12:15], s38 offen offset:2048 lds\n"
    "s_waitcnt lgkmcnt(12)\n"
    "v_mfma_f32_16x16x32_bf16 v[112:115], a[28:31], a[0:3], v[112:115]\n"
    "v_mfma_f32_16x16x32_bf16 v[116:119], a[28:31], a[4:7], v[116:119]\n"
    "v_mfma_f32_16x16x32_bf16 v[120:123], a[28:31], a[8:11], v[120:123]\n"
    "v_mfma_f32_16x16x32_bf16 v[124:127], a[28:31], a[12:15], v[124:127]\n"
    "buffer_load_dwordx4 v223, s[12:15], s38 offen offset:3072 lds\n"
    "s_waitcnt vmcnt(16)\n"
    "s_barrier\n"
    "s_waitcnt lgkmcnt(3)\n"
    "v_mfma_f32_16x16x32_bf16 v[64:67], a[48:51], a[32:35], v[64:67]\n"
    "ds_read_b128 a[0:3], v194 offset:0\n"
    "v_mfma_f32_16x16x32_bf16 v[68:71], a[48:51], a[36:39], v[68:71]\n"
    "ds_read_b64_tr_b16 a[16:17], v48 offset:16384\n"
    "v_mfma_f32_16x16x32_bf16 v[80:83], a[52:55], a[32:35], v[80:83]\n"
    "ds_read_b64_tr_b16 a[18:19], v48 offset:16896\n"
    "v_mfma_f32_16x16x32_bf16 v[84:87], a[52:55], a[36:39], v[84:87]\n"
    "ds_read_b128 a[4:7], v194 offset:2048\n"
    "s_add_u32 m0, s36, 0\n"
    "s_nop 0\n"
    "buffer_load_dwordx4 v224, s[16:19], 0 offen nt lds\n"
    "v_mfma_f32_16x16x32_bf16 v[72:75], a[48:51], a[40:43], v[72:75]\n"
    "ds_read_b64_tr_b16 a[20:21], v49 offset:16384\n"
    "v_mfma_f32_16x16x32_bf16 v[88:91], a[52:55], a[40:43], v[88:91]\n"
    "ds_read_b64_tr_b16 a[22:23], v49 offset:16896\n"
    "v_mfma_f32_16x16x32_bf16 v[96:99], a[56:59], a[32:35], v[96:99]\n"
    "ds_read_b128 a[8:11], v200 offset:0\n"
    "v_mfma_f32_16x16x32_bf16 v[100:103], a[56:59], a[36:39], v[100:103]\n"
    "ds_read_b64_tr_b16 a[24:25], v50 offset:16384\n"
    "buffer_load_dwordx4 v225, s[16:19], 0 offen offset:1024 nt lds\n"
    "v_mfma_f32_16x16x32_bf16 v[104:107], a[56:59], a[40:43], v[104:107]\n"
    "ds_read_b64_tr_b16 a[26:27], v50 offset:16896\n"
    "s_waitcnt lgkmcnt(11)\n"
    "v_mfma_f32_16x16x32_bf16 v[76:79], a[48:51], a[44:47], v[76:79]\n"
    "ds_read_b128 a[12:15], v200 offset:2048\n"
    "v_mfma_f32_16x16x32_bf16 v[92:95], a[52:55], a[44:47], v[92:95]\n"
    "ds_read_b64_tr_b16 a[28:29], v51 offset:16384\n"
    "v_mfma_f32_16x16x32_bf16 v[108:111], a[56:59], a[44:47], v[108:111]\n"
    "ds_read_b64_tr_b16 a[30:31], v51 offset:16896\n"
    "buffer_load_dwordx4 v226, s[16:19], 0 offen offset:2048 nt lds\n"
    "s_waitcnt lgkmcnt(12)\n"
    "v_mfma_f32_16x16x32_bf16 v[112:115], a[60:63], a[32:35], v[112:115]\n"
    "v_mfma_f32_16x16x32_bf16 v[116:119], a[60:63], a[36:39], v[116:119]\n"
    "v_mfma_f32_16x16x32_bf16 v[120:123], a[60:63], a[40:43], v[120:123]\n"
    "v_mfma_f32_16x16x32_bf16 v[124:127], a[60:63], a[44:47], v[124:127]\n"
    "buffer_load_dwordx4 v227, s[16:19], 0 offen offset:3072 nt lds\n"
    "; ================ period 7 ================\n"
    "s_waitcnt lgkmcnt(3)\n"
    "v_mfma_f32_16x16x32_bf16 v[64:67], a[16:19], a[0:3], v[64:67]\n"
    "ds_read_b128 a[32:35], v195 offset:0\n"
    "v_mfma_f32_16x16x32_bf16 v[68:71], a[16:19], a[4:7], v[68:71]\n"
    "ds_read_b64_tr_b16 a[48:49], v48 offset:20480\n"
    "v_mfma_f32_16x16x32_bf16 v[80:83], a[20:23], a[0:3], v[80:83]\n"
    "ds_read_b64_tr_b16 a[50:51], v48 offset:20992\n"
    "v_mfma_f32_16x16x32_bf16 v[84:87], a[20:23], a[4:7], v[84:87]\n"
    "ds_read_b128 a[36:39], v195 offset:2048\n"
    "s_add_u32 m0, s36, 16384\n"
    "v_xor_b32 v224, 0x40000, v224\n"
    "v_xor_b32 v225, 0x40000, v225\n"
    "v_xor_b32 v226, 0x40000, v226\n"
    "v_xor_b32 v227, 0x40000, v227\n"
    "buffer_load_dwordx4 v224, s[16:19], 0 offen nt lds\n"
    "v_mfma_f32_16x16x32_bf16 v[72:75], a[16:19], a[8:11], v[72:75]\n"
    "ds_read_b64_tr_b16 a[52:53], v49 offset:20480\n"
    "v_mfma_f32_16x16x32_bf16 v[88:91], a[20:23], a[8:11], v[88:91]\n"
    "ds_read_b64_tr_b16 a[54:55], v49 offset:20992\n"
    "v_mfma_f32_16x16x32_bf16 v[96:99], a[24:27], a[0:3], v[96:99]\n"
    "ds_read_b128 a[40:43], v201 offset:0\n"
    "v_mfma_f32_16x16x32_bf16 v[100:103], a[24:27], a[4:7], v[100:103]\n"
    "ds_read_b64_tr_b16 a[56:57], v50 offset:20480\n"
    "buffer_load_dwordx4 v225, s[16:19], 0 offen offset:1024 nt lds\n"
    "v_mfma_f32_16x16x32_bf16 v[104:107], a[24:27], a[8:11], v[104:107]\n"
    "ds_read_b64_tr_b16 a[58:59], v50 offset:20992\n"
    "s_waitcnt lgkmcnt(11)\n"
    "v_mfma_f32_16x16x32_bf16 v[76:79], a[16:19], a[12:15], v[76:79]\n"
    "ds_read_b128 a[44:47], v201 offset:2048\n"
    "v_mfma_f32_16x16x32_bf16 v[92:95], a[20:23], a[12:15], v[92:95]\n"
    "ds_read_b64_tr_b16 a[60:61], v51 offset:20480\n"
    "v_mfma_f32_16x16x32_bf16 v[108:111], a[24:27], a[12:15], v[108:111]\n"
    "ds_read_b64_tr_b16 a[62:63], v51 offset:20992\n"
    "buffer_load_dwordx4 v226, s[16:19], 0 offen offset:2048 nt lds\n"
    "s_waitcnt lgkmcnt(12)\n"
    "v_mfma_f32_16x16x32_bf16 v[112:115], a[28:31], a[0:3], v[112:115]\n"
    "v_mfma_f32_16x16x32_bf16 v[116:119], a[28:31], a[4:7], v[116:119]\n"
    "v_mfma_f32_16x16x32_bf16 v[120:123], a[28:31], a[8:11], v[120:123]\n"
    "v_mfma_f32_16x16x32_bf16 v[124:127], a[28:31], a[12:15], v[124:127]\n"
    "buffer_load_dwordx4 v227, s[16:19], 0 offen offset:3072 nt lds\n"
    "s_waitcnt vmcnt(16)\n"
    "s_barrier\n"
    "s_waitcnt lgkmcnt(3)\n"
    "v_mfma_f32_16x16x32_bf16 v[64:67], a[48:51], a[32:35], v[64:67]\n"
    "ds_read_b128 a[0:3], v194 offset:32768\n"
    "v_mfma_f32_16x16x32_bf16 v[68:71], a[48:51], a[36:39], v[68:71]\n"
    "ds_read_b64_tr_b16 a[16:17], v48 offset:49152\n"
    "v_mfma_f32_16x16x32_bf16 v[80:83], a[52:55], a[32:35], v[80:83]\n"
    "ds_read_b64_tr_b16 a[18:19], v48 offset:49664\n"
    "v_mfma_f32_16x16x32_bf16 v[84:87], a[52:55], a[36:39], v[84:87]\n"
    "ds_read_b128 a[4:7], v194 offset:34816\n"
    "s_add_u32 m0, s36, 32768\n"
    "s_bfe_u32 s38, s57, 0x40008\n"
    "s_lshl_b32 s38, s38, 7\n"
    "buffer_load_dwordx4 v216, s[8:11], s38 offen lds\n"
    "v_mfma_f32_16x16x32_bf16 v[72:75], a[48:51], a[40:43], v[72:75]\n"
    "ds_read_b64_tr_b16 a[20:21], v49 offset:49152\n"
    "v_mfma_f32_16x16x32_bf16 v[88:91], a[52:55], a[40:43], v[88:91]\n"
    "ds_read_b64_tr_b16 a[22:23], v49 offset:49664\n"
    "v_mfma_f32_16x16x32_bf16 v[96:99], a[56:59], a[32:35], v[96:99]\n"
    "ds_read_b128 a[8:11], v200 offset:32768\n"
    "v_mfma_f32_16x16x32_bf16 v[100:103], a[56:59], a[36:39], v[100:103]\n"
    "ds_read_b64_tr_b16 a[24:25], v50 offset:49152\n"
    "buffer_load_dwordx4 v217, s[8:11], s38 offen offset:1024 lds\n"
    "v_mfma_f32_16x16x32_bf16 v[104:107], a[56:59], a[40:43], v[104:107]\n"
    "ds_read_b64_tr_b16 a[26:27], v50 offset:49664\n"
    "s_waitcnt lgkmcnt(11)\n"
    "v_mfma_f32_16x16x32_bf16 v[76:79], a[48:51], a[44:47], v[76:79]\n"
    "ds_read_b128 a[12:15], v200 offset:34816\n"
    "v_mfma_f32_16x16x32_bf16 v[92:95], a[52:55], a[44:47], v[92:95]\n"
    "ds_read_b64_tr_b16 a[28:29], v51 offset:49152\n"
    "v_mfma_f32_16x16x32_bf16 v[108:111], a[56:59], a[44:47], v[108:111]\n"
    "ds_read_b64_tr_b16 a[30:31], v51 offset:49664\n"
    "buffer_load_dwordx4 v218, s[8:11], s38 offen offset:2048 lds\n"
    "s_waitcnt lgkmcnt(12)\n"
    "v_mfma_f32_16x16x32_bf16 v[112:115], a[60:63], a[32:35], v[112:115]\n"
    "v_mfma_f32_16x16x32_bf16 v[116:119], a[60:63], a[36:39], v[116:119]\n"
    "v_mfma_f32_16x16x32_bf16 v[120:123], a[60:63], a[40:43], v[120:123]\n"
    "v_mfma_f32_16x16x32_bf16 v[124:127], a[60:63], a[44:47], v[124:127]\n"
    "buffer_load_dwordx4 v219, s[8:11], s38 offen offset:3072 lds\n"
    "; ================ period 8 ================\n"
    "s_waitcnt lgkmcnt(3)\n"
    "v_mfma_f32_16x16x32_bf16 v[64:67], a[16:19], a[0:3], v[64:67]\n"
    "ds_read_b128 a[32:35], v195 offset:32768\n"
    "v_mfma_f32_16x16x32_bf16 v[68:71], a[16:19], a[4:7], v[68:71]\n"
    "ds_read_b64_tr_b16 a[48:49], v48 offset:53248\n"
    "v_mfma_f32_16x16x32_bf16 v[80:83], a[20:23], a[0:3], v[80:83]\n"
    "ds_read_b64_tr_b16 a[50:51], v48 offset:53760\n"
    "v_mfma_f32_16x16x32_bf16 v[84:87], a[20:23], a[4:7], v[84:87]\n"
    "ds_read_b128 a[36:39], v195 offset:34816\n"
    "s_add_u32 m0, s36, 49152\n"
    "s_bfe_u32 s38, s57, 0x40008\n"
    "s_lshl_b32 s38, s38, 19\n"
    "buffer_load_dwordx4 v220, s[12:15], s38 offen lds\n"
    "v_mfma_f32_16x16x32_bf16 v[72:75], a[16:19], a[8:11], v[72:75]\n"
    "ds_read_b64_tr_b16 a[52:53], v49 offset:53248\n"
    "v_mfma_f32_16x16x32_bf16 v[88:91], a[20:23], a[8:11], v[88:91]\n"
    "ds_read_b64_tr_b16 a[54:55], v49 offset:53760\n"
    "v_mfma_f32_16x16x32_bf16 v[96:99], a[24:27], a[0:3], v[96:99]\n"
    "ds_read_b128 a[40:43], v201 offset:32768\n"
    "v_mfma_f32_16x16x32_bf16 v[100:103], a[24:27], a[4:7], v[100:103]\n"
    "ds_read_b64_tr_b16 a[56:57], v50 offset:53248\n"
    "buffer_load_dwordx4 v221, s[12:15], s38 offen offset:1024 lds\n"
    "v_mfma_f32_16x16x32_bf16 v[104:107], a[24:27], a[8:11], v[104:107]\n"
    "ds_read_b64_tr_b16 a[58:59], v50 offset:53760\n"
    "s_waitcnt lgkmcnt(11)\n"
    "v_mfma_f32_16x16x32_bf16 v[76:79], a[16:19], a[12:15], v[76:79]\n"
    "ds_read_b128 a[44:47], v201 offset:34816\n"
    "v_mfma_f32_16x16x32_bf16 v[92:95], a[20:23], a[12:15], v[92:95]\n"
    "ds_read_b64_tr_b16 a[60:61], v51 offset:53248\n"
    "v_mfma_f32_16x16x32_bf16 v[108:111], a[24:27], a[12:15], v[108:111]\n"
    "ds_read_b64_tr_b16 a[62:63], v51 offset:53760\n"
    "buffer_load_dwordx4 v222, s[12:15], s38 offen offset:2048 lds\n"
    "s_waitcnt lgkmcnt(12)\n"
    "v_mfma_f32_16x16x32_bf16 v[112:115], a[28:31], a[0:3], v[112:115]\n"
    "v_mfma_f32_16x16x32_bf16 v[116:119], a[28:31], a[4:7], v[116:119]\n"
    "v_mfma_f32_16x16x32_bf16 v[120:123], a[28:31], a[8:11], v[120:123]\n"
    "v_mfma_f32_16x16x32_bf16 v[124:127], a[28:31], a[12:15], v[124:127]\n"
    "buffer_load_dwordx4 v223, s[12:15], s38 offen offset:3072 lds\n"
    "s_waitcnt vmcnt(16)\n"
    "s_barrier\n"
    "s_waitcnt lgkmcnt(3)\n"
    "v_mfma_f32_16x16x32_bf16 v[64:67], a[48:51], a[32:35], v[64:67]\n"
    "ds_read_b128 a[0:3], v196 offset:0\n"
    "v_mfma_f32_16x16x32_bf16 v[68:71], a[48:51], a[36:39], v[68:71]\n"
    "ds_read_b64_tr_b16 a[16:17], v52 offset:16384\n"
    "v_mfma_f32_16x16x32_bf16 v[80:83], a[52:55], a[32:35], v[80:83]\n"
    "ds_read_b64_tr_b16 a[18:19], v52 offset:16896\n"
    "v_mfma_f32_16x16x32_bf16 v[84:87], a[52:55], a[36:39], v[84:87]\n"
    "ds_read_b128 a[4:7], v196 offset:2048\n"
    "s_add_u32 m0, s36, 65536\n"
    "s_bfe_u32 s38, s57, 0x4000c\n"
    "s_lshl_b32 s38, s38, 7\n"
    "buffer_load_dwordx4 v216, s[8:11], s38 offen lds\n"
    "v_mfma_f32_16x16x32_bf16 v[72:75], a[48:51], a[40:43], v[72:75]\n"
    "ds_read_b64_tr_b16 a[20:21], v53 offset:16384\n"
    "v_mfma_f32_16x16x32_bf16 v[88:91], a[52:55], a[40:43], v[88:91]\n"
    "ds_read_b64_tr_b16 a[22:23], v53 offset:16896\n"
    "v_mfma_f32_16x16x32_bf16 v[96:99], a[56:59], a[32:35], v[96:99]\n"
    "ds_read_b128 a[8:11], v202 offset:0\n"
    "v_mfma_f32_16x16x32_bf16 v[100:103], a[56:59], a[36:39], v[100:103]\n"
    "ds_read_b64_tr_b16 a[24:25], v54 offset:16384\n"
    "buffer_load_dwordx4 v217, s[8:11], s38 offen offset:1024 lds\n"
    "v_mfma_f32_16x16x32_bf16 v[104:107], a[56:59], a[40:43], v[104:107]\n"
    "ds_read_b64_tr_b16 a[26:27], v54 offset:16896\n"
    "s_waitcnt lgkmcnt(11)\n"
    "v_mfma_f32_16x16x32_bf16 v[76:79], a[48:51], a[44:47], v[76:79]\n"
    "ds_read_b128 a[12:15], v202 offset:2048\n"
    "v_mfma_f32_16x16x32_bf16 v[92:95], a[52:55], a[44:47], v[92:95]\n"
    "ds_read_b64_tr_b16 a[28:29], v55 offset:16384\n"
    "v_mfma_f32_16x16x32_bf16 v[108:111], a[56:59], a[44:47], v[108:111]\n"
    "ds_read_b64_tr_b16 a[30:31], v55 offset:16896\n"
    "buffer_load_dwordx4 v218, s[8:11], s38 offen offset:2048 lds\n"
    "s_waitcnt lgkmcnt(12)\n"
    "v_mfma_f32_16x16x32_bf16 v[112:115], a[60:63], a[32:35], v[112:115]\n"
    "v_mfma_f32_16x16x32_bf16 v[116:119], a[60:63], a[36:39], v[116:119]\n"
    "v_mfma_f32_16x16x32_bf16 v[120:123], a[60:63], a[40:43], v[120:123]\n"
    "v_mfma_f32_16x16x32_bf16 v[124:127], a[60:63], a[44:47], v[124:127]\n"
    "buffer_load_dwordx4 v219, s[8:11], s38 offen offset:3072 lds\n"
    "; ================ period 9 ================\n"
    "s_waitcnt lgkmcnt(3)\n"
    "v_mfma_f32_16x16x32_bf16 v[64:67], a[16:19], a[0:3], v[64:67]\n"
    "ds_read_b128 a[32:35], v197 offset:0\n"
    "v_mfma_f32_16x16x32_bf16 v[68:71], a[16:19], a[4:7], v[68:71]\n"
    "ds_read_b64_tr_b16 a[48:49], v52 offset:20480\n"
    "v_mfma_f32_16x16x32_bf16 v[80:83], a[20:23], a[0:3], v[80:83]\n"
    "ds_read_b64_tr_b16 a[50:51], v52 offset:20992\n"
    "v_mfma_f32_16x16x32_bf16 v[84:87], a[20:23], a[4:7], v[84:87]\n"
    "ds_read_b128 a[36:39], v197 offset:2048\n"
    "s_add_u32 m0, s36, 81920\n"
    "s_bfe_u32 s38, s57, 0x4000c\n"
    "s_lshl_b32 s38, s38, 19\n"
    "buffer_load_dwordx4 v220, s[12:15], s38 offen lds\n"
    "v_mfma_f32_16x16x32_bf16 v[72:75], a[16:19], a[8:11], v[72:75]\n"
    "ds_read_b64_tr_b16 a[52:53], v53 offset:20480\n"
    "v_mfma_f32_16x16x32_bf16 v[88:91], a[20:23], a[8:11], v[88:91]\n"
    "ds_read_b64_tr_b16 a[54:55], v53 offset:20992\n"
    "v_mfma_f32_16x16x32_bf16 v[96:99], a[24:27], a[0:3], v[96:99]\n"
    "ds_read_b128 a[40:43], v203 offset:0\n"
    "v_mfma_f32_16x16x32_bf16 v[100:103], a[24:27], a[4:7], v[100:103]\n"
    "ds_read_b64_tr_b16 a[56:57], v54 offset:20480\n"
    "buffer_load_dwordx4 v221, s[12:15], s38 offen offset:1024 lds\n"
    "v_mfma_f32_16x16x32_bf16 v[104:107], a[24:27], a[8:11], v[104:107]\n"
    "ds_read_b64_tr_b16 a[58:59], v54 offset:20992\n"
    "s_waitcnt lgkmcnt(11)\n"
    "v_mfma_f32_16x16x32_bf16 v[76:79], a[16:19], a[12:15], v[76:79]\n"
    "ds_read_b128 a[44:47], v203 offset:2048\n"
    "v_mfma_f32_16x16x32_bf16 v[92:95], a[20:23], a[12:15], v[92:95]\n"
    "ds_read_b64_tr_b16 a[60:61], v55 offset:20480\n"
    "v_mfma_f32_16x16x32_bf16 v[108:111], a[24:27], a[12:15], v[108:111]\n"
    "ds_read_b64_tr_b16 a[62:63], v55 offset:20992\n"
    "buffer_load_dwordx4 v222, s[12:15], s38 offen offset:2048 lds\n"
    "s_waitcnt lgkmcnt(12)\n"
    "v_mfma_f32_16x16x32_bf16 v[112:115], a[28:31], a[0:3], v[112:115]\n"
    "v_mfma_f32_16x16x32_bf16 v[116:119], a[28:31], a[4:7], v[116:119]\n"
    "v_mfma_f32_16x16x32_bf16 v[120:123], a[28:31], a[8:11], v[120:123]\n"
    "v_mfma_f32_16x16x32_bf16 v[124:127], a[28:31], a[12:15], v[124:127]\n"
    "buffer_load_dwordx4 v223, s[12:15], s38 offen offset:3072 lds\n"
    "s_waitcnt vmcnt(8)\n"
    "s_barrier\n"
    "s_waitcnt lgkmcnt(3)\n"
    "v_mfma_f32_16x16x32_bf16 v[64:67], a[48:51], a[32:35], v[64:67]\n"
    "ds_read_b128 a[0:3], v192 offset:32768\n"
    "v_mfma_f32_16x16x32_bf16 v[68:71], a[48:51], a[36:39], v[68:71]\n"
    "ds_read_b64_tr_b16 a[16:17], v6 offset:49152\n"
    "v_mfma_f32_16x16x32_bf16 v[80:83], a[52:55], a[32:35], v[80:83]\n"
    "ds_read_b64_tr_b16 a[18:19], v6 offset:49664\n"
    "v_mfma_f32_16x16x32_bf16 v[84:87], a[52:55], a[36:39], v[84:87]\n"
    "ds_read_b128 a[4:7], v192 offset:34816\n"
    "s_add_u32 m0, s36, 98304\n"
    "s_bfe_u32 s38, s57, 0x40010\n"
    "s_lshl_b32 s38, s38, 7\n"
    "buffer_load_dwordx4 v216, s[8:11], s38 offen lds\n"
    "v_mfma_f32_16x16x32_bf16 v[72:75], a[48:51], a[40:43], v[72:75]\n"
    "ds_read_b64_tr_b16 a[20:21], v7 offset:49152\n"
    "v_mfma_f32_16x16x32_bf16 v[88:91], a[52:55], a[40:43], v[88:91]\n"
    "ds_read_b64_tr_b16 a[22:23], v7 offset:49664\n"
    "v_mfma_f32_16x16x32_bf16 v[96:99], a[56:59], a[32:35], v[96:99]\n"
    "ds_read_b128 a[8:11], v198 offset:32768\n"
    "v_mfma_f32_16x16x32_bf16 v[100:103], a[56:59], a[36:39], v[100:103]\n"
    "ds_read_b64_tr_b16 a[24:25], v8 offset:49152\n"
    "buffer_load_dwordx4 v217, s[8:11], s38 offen offset:1024 lds\n"
    "v_mfma_f32_16x16x32_bf16 v[104:107], a[56:59], a[40:43], v[104:107]\n"
    "ds_read_b64_tr_b16 a[26:27], v8 offset:49664\n"
    "s_waitcnt lgkmcnt(11)\n"
    "v_mfma_f32_16x16x32_bf16 v[76:79], a[48:51], a[44:47], v[76:79]\n"
    "ds_read_b128 a[12:15], v198 offset:34816\n"
    "v_mfma_f32_16x16x32_bf16 v[92:95], a[52:55], a[44:47], v[92:95]\n"
    "ds_read_b64_tr_b16 a[28:29], v9 offset:49152\n"
    "v_mfma_f32_16x16x32_bf16 v[108:111], a[56:59], a[44:47], v[108:111]\n"
    "ds_read_b64_tr_b16 a[30:31], v9 offset:49664\n"
    "buffer_load_dwordx4 v218, s[8:11], s38 offen offset:2048 lds\n"
    "s_waitcnt lgkmcnt(12)\n"
    "v_mfma_f32_16x16x32_bf16 v[112:115], a[60:63], a[32:35], v[112:115]\n"
    "v_mfma_f32_16x16x32_bf16 v[116:119], a[60:63], a[36:39], v[116:119]\n"
    "v_mfma_f32_16x16x32_bf16 v[120:123], a[60:63], a[40:43], v[120:123]\n"
    "v_mfma_f32_16x16x32_bf16 v[124:127], a[60:63], a[44:47], v[124:127]\n"
    "buffer_load_dwordx4 v219, s[8:11], s38 offen offset:3072 lds\n"
    "; ================ period 10 ================\n"
    "s_waitcnt lgkmcnt(3)\n"
    "v_mfma_f32_16x16x32_bf16 v[64:67], a[16:19], a[0:3], v[64:67]\n"
    "ds_read_b128 a[32:35], v193 offset:32768\n"
    "ds_read_b64 a[224:225], v204 offset:0\n"
    "v_mfma_f32_16x16x32_bf16 v[68:71], a[16:19], a[4:7], v[68:71]\n"
    "ds_read_b64_tr_b16 a[48:49], v6 offset:53248\n"
    "ds_read_b64 a[226:227], v205 offset:0\n"
    "v_mfma_f32_16x16x32_bf16 v[80:83], a[20:23], a[0:3], v[80:83]\n"
    "ds_read_b64_tr_b16 a[50:51], v6 offset:53760\n"
    "ds_read_b64 a[228:229], v206 offset:0\n"
    "v_mfma_f32_16x16x32_bf16 v[84:87], a[20:23], a[4:7], v[84:87]\n"
    "ds_read_b128 a[36:39], v193 offset:34816\n"
    "s_add_u32 m0, s36, 114688\n"
    "s_bfe_u32 s38, s57, 0x40010\n"
    "s_lshl_b32 s38, s38, 19\n"
    "buffer_load_dwordx4 v220, s[12:15], s38 offen lds\n"
    "ds_read_b64 a[230:231], v207 offset:0\n"
    "v_mfma_f32_16x16x32_bf16 v[72:75], a[16:19], a[8:11], v[72:75]\n"
    "ds_read_b64_tr_b16 a[52:53], v7 offset:53248\n"
    "ds_read_b64 a[232:233], v204 offset:2048\n"
    "v_mfma_f32_16x16x32_bf16 v[88:91], a[20:23], a[8:11], v[88:91]\n"
    "ds_read_b64_tr_b16 a[54:55], v7 offset:53760\n"
    "ds_read_b64 a[234:235], v205 offset:2048\n"
    "v_mfma_f32_16x16x32_bf16 v[96:99], a[24:27], a[0:3], v[96:99]\n"
    "s_waitcnt lgkmcnt(8)\n"
    "ds_read_b128 a[40:43], v199 offset:32768\n"
    "ds_read_b64 a[236:237], v206 offset:2048\n"
    "v_mfma_f32_16x16x32_bf16 v[100:103], a[24:27], a[4:7], v[100:103]\n"
    "ds_read_b64_tr_b16 a[56:57], v8 offset:53248\n"
    "buffer_load_dwordx4 v221, s[12:15], s38 offen offset:1024 lds\n"
    "ds_read_b64 a[238:239], v207 offset:2048\n"
    "v_mfma_f32_16x16x32_bf16 v[104:107], a[24:27], a[8:11], v[104:107]\n"
    "ds_read_b64_tr_b16 a[58:59], v8 offset:53760\n"
    "ds_read_b64 a[240:241], v204 offset:16384\n"
    "v_mfma_f32_16x16x32_bf16 v[76:79], a[16:19], a[12:15], v[76:79]\n"
    "ds_read_b128 a[44:47], v199 offset:34816\n"
    "s_waitcnt lgkmcnt(8)\n"
    "ds_read_b64 a[242:243], v205 offset:16384\n"
    "v_mfma_f32_16x16x32_bf16 v[92:95], a[20:23], a[12:15], v[92:95]\n"
    "ds_read_b64_tr_b16 a[60:61], v9 offset:53248\n"
    "ds_read_b64 a[244:245], v206 offset:16384\n"
    "v_mfma_f32_16x16x32_bf16 v[108:111], a[24:27], a[12:15], v[108:111]\n"
    "ds_read_b64_tr_b16 a[62:63], v9 offset:53760\n"
    "buffer_load_dwordx4 v222, s[12:15], s38 offen offset:2048 lds\n"
    "ds_read_b64 a[246:247], v207 offset:16384\n"
    "v_mfma_f32_16x16x32_bf16 v[112:115], a[28:31], a[0:3], v[112:115]\n"
    "ds_read_b64 a[248:249], v204 offset:18432\n"
    "v_mfma_f32_16x16x32_bf16 v[116:119], a[28:31], a[4:7], v[116:119]\n"
    "ds_read_b64 a[250:251], v205 offset:18432\n"
    "v_mfma_f32_16x16x32_bf16 v[120:123], a[28:31], a[8:11], v[120:123]\n"
    "s_waitcnt lgkmcnt(8)\n"
    "ds_read_b64 a[252:253], v206 offset:18432\n"
    "v_mfma_f32_16x16x32_bf16 v[124:127], a[28:31], a[12:15], v[124:127]\n"
    "buffer_load_dwordx4 v223, s[12:15], s38 offen offset:3072 lds\n"
    "ds_read_b64 a[254:255], v207 offset:18432\n"
    "s_waitcnt vmcnt(8)\n"
    "s_barrier\n"
    "s_waitcnt lgkmcnt(3)\n"
    "v_mfma_f32_16x16x32_bf16 v[64:67], a[48:51], a[32:35], v[64:67]\n"
    "ds_read_b128 a[0:3], v194 offset:0\n"
    "v_mfma_f32_16x16x32_bf16 v[68:71], a[48:51], a[36:39], v[68:71]\n"
    "ds_read_b64_tr_b16 a[16:17], v48 offset:16384\n"
    "v_mfma_f32_16x16x32_bf16 v[80:83], a[52:55], a[32:35], v[80:83]\n"
    "ds_read_b64_tr_b16 a[18:19], v48 offset:16896\n"
    "v_mfma_f32_16x16x32_bf16 v[84:87], a[52:55], a[36:39], v[84:87]\n"
    "ds_read_b128 a[4:7], v194 offset:2048\n"
    "s_add_u32 m0, s36, 131072\n"
    "s_bfe_u32 s38, s57, 0x40014\n"
    "s_lshl_b32 s38, s38, 7\n"
    "buffer_load_dwordx4 v216, s[8:11], s38 offen lds\n"
    "v_mfma_f32_16x16x32_bf16 v[72:75], a[48:51], a[40:43], v[72:75]\n"
    "ds_read_b64_tr_b16 a[20:21], v49 offset:16384\n"
    "v_mfma_f32_16x16x32_bf16 v[88:91], a[52:55], a[40:43], v[88:91]\n"
    "ds_read_b64_tr_b16 a[22:23], v49 offset:16896\n"
    "v_mfma_f32_16x16x32_bf16 v[96:99], a[56:59], a[32:35], v[96:99]\n"
    "ds_read_b128 a[8:11], v200 offset:0\n"
    "v_mfma_f32_16x16x32_bf16 v[100:103], a[56:59], a[36:39], v[100:103]\n"
    "ds_read_b64_tr_b16 a[24:25], v50 offset:16384\n"
    "buffer_load_dwordx4 v217, s[8:11], s38 offen offset:1024 lds\n"
    "v_mfma_f32_16x16x32_bf16 v[104:107], a[56:59], a[40:43], v[104:107]\n"
    "ds_read_b64_tr_b16 a[26:27], v50 offset:16896\n"
    "v_mfma_f32_16x16x32_bf16 v[76:79], a[48:51], a[44:47], v[76:79]\n"
    "ds_read_b128 a[12:15], v200 offset:2048\n"
    "v_mfma_f32_16x16x32_bf16 v[92:95], a[52:55], a[44:47], v[92:95]\n"
    "ds_read_b64_tr_b16 a[28:29], v51 offset:16384\n"
    "v_mfma_f32_16x16x32_bf16 v[108:111], a[56:59], a[44:47], v[108:111]\n"
    "ds_read_b64_tr_b16 a[30:31], v51 offset:16896\n"
    "buffer_load_dwordx4 v218, s[8:11], s38 offen offset:2048 lds\n"
    "v_mfma_f32_16x16x32_bf16 v[112:115], a[60:63], a[32:35], v[112:115]\n"
    "v_mfma_f32_16x16x32_bf16 v[116:119], a[60:63], a[36:39], v[116:119]\n"
    "v_mfma_f32_16x16x32_bf16 v[120:123], a[60:63], a[40:43], v[120:123]\n"
    "v_mfma_f32_16x16x32_bf16 v[124:127], a[60:63], a[44:47], v[124:127]\n"
    "buffer_load_dwordx4 v219, s[8:11], s38 offen offset:3072 lds\n"
    "; ================ period 11 ================\n"
    "s_waitcnt lgkmcnt(3)\n"
    "v_mfma_f32_16x16x32_bf16 v[64:67], a[16:19], a[0:3], v[64:67]\n"
    "ds_read_b128 a[32:35], v195 offset:0\n"
    "v_mfma_f32_16x16x32_bf16 v[68:71], a[16:19], a[4:7], v[68:71]\n"
    "ds_read_b64_tr_b16 a[48:49], v48 offset:20480\n"
    "v_mfma_f32_16x16x32_bf16 v[80:83], a[20:23], a[0:3], v[80:83]\n"
    "ds_read_b64_tr_b16 a[50:51], v48 offset:20992\n"
    "v_mfma_f32_16x16x32_bf16 v[84:87], a[20:23], a[4:7], v[84:87]\n"
    "ds_read_b128 a[36:39], v195 offset:2048\n"
    "s_add_u32 m0, s36, 147456\n"
    "s_bfe_u32 s38, s57, 0x40014\n"
    "s_lshl_b32 s38, s38, 19\n"
    "buffer_load_dwordx4 v220, s[12:15], s38 offen lds\n"
    "v_mfma_f32_16x16x32_bf16 v[72:75], a[16:19], a[8:11], v[72:75]\n"
    "ds_read_b64_tr_b16 a[52:53], v49 offset:20480\n"
    "v_mfma_f32_16x16x32_bf16 v[88:91], a[20:23], a[8:11], v[88:91]\n"
    "ds_read_b64_tr_b16 a[54:55], v49 offset:20992\n"
    "v_mfma_f32_16x16x32_bf16 v[96:99], a[24:27], a[0:3], v[96:99]\n"
    "ds_read_b128 a[40:43], v201 offset:0\n"
    "v_mfma_f32_16x16x32_bf16 v[100:103], a[24:27], a[4:7], v[100:103]\n"
    "ds_read_b64_tr_b16 a[56:57], v50 offset:20480\n"
    "buffer_load_dwordx4 v221, s[12:15], s38 offen offset:1024 lds\n"
    "v_mfma_f32_16x16x32_bf16 v[104:107], a[24:27], a[8:11], v[104:107]\n"
    "ds_read_b64_tr_b16 a[58:59], v50 offset:20992\n"
    "s_waitcnt lgkmcnt(11)\n"
    "v_mfma_f32_16x16x32_bf16 v[76:79], a[16:19], a[12:15], v[76:79]\n"
    "ds_read_b128 a[44:47], v201 offset:2048\n"
    "v_mfma_f32_16x16x32_bf16 v[92:95], a[20:23], a[12:15], v[92:95]\n"
    "ds_read_b64_tr_b16 a[60:61], v51 offset:20480\n"
    "v_mfma_f32_16x16x32_bf16 v[108:111], a[24:27], a[12:15], v[108:111]\n"
    "ds_read_b64_tr_b16 a[62:63], v51 offset:20992\n"
    "buffer_load_dwordx4 v222, s[12:15], s38 offen offset:2048 lds\n"
    "s_waitcnt lgkmcnt(12)\n"
    "v_mfma_f32_16x16x32_bf16 v[112:115], a[28:31], a[0:3], v[112:115]\n"
    "v_mfma_f32_16x16x32_bf16 v[116:119], a[28:31], a[4:7], v[116:119]\n"
    "v_mfma_f32_16x16x32_bf16 v[120:123], a[28:31], a[8:11], v[120:123]\n"
    "v_mfma_f32_16x16x32_bf16 v[124:127], a[28:31], a[12:15], v[124:127]\n"
    "buffer_load_dwordx4 v223, s[12:15], s38 offen offset:3072 lds\n"
    "s_waitcnt vmcnt(8)\n"
    "s_barrier\n"
    "s_waitcnt lgkmcnt(3)\n"
    "v_mfma_f32_16x16x32_bf16 v[64:67], a[48:51], a[32:35], v[64:67]\n"
    "ds_read_b128 a[0:3], v194 offset:32768\n"
    "v_mfma_f32_16x16x32_bf16 v[68:71], a[48:51], a[36:39], v[68:71]\n"
    "ds_read_b64_tr_b16 a[16:17], v48 offset:49152\n"
    "s_add_u32 m0, s36, 0\n"
    "s_bfe_u32 s38, s57, 0x40018\n"
    "s_lshl_b32 s38, s38, 7\n"
    "buffer_load_dwordx4 v216, s[8:11], s38 offen lds\n"
    "v_mfma_f32_16x16x32_bf16 v[80:83], a[52:55], a[32:35], v[80:83]\n"
    "ds_read_b64_tr_b16 a[18:19], v48 offset:49664\n"
    "v_mfma_f32_16x16x32_bf16 v[84:87], a[52:55], a[36:39], v[84:87]\n"
    "ds_read_b128 a[4:7], v194 offset:34816\n"
    "buffer_load_dwordx4 v217, s[8:11], s38 offen offset:1024 lds\n"
    "buffer_load_dwordx4 v218, s[8:11], s38 offen offset:2048 lds\n"
    "v_mfma_f32_16x16x32_bf16 v[72:75], a[48:51], a[40:43], v[72:75]\n"
    "ds_read_b64_tr_b16 a[20:21], v49 offset:49152\n"
    "v_mfma_f32_16x16x32_bf16 v[88:91], a[52:55], a[40:43], v[88:91]\n"
    "ds_read_b64_tr_b16 a[22:23], v49 offset:49664\n"
    "buffer_load_dwordx4 v219, s[8:11], s38 offen offset:3072 lds\n"
    "v_mfma_f32_16x16x32_bf16 v[96:99], a[56:59], a[32:35], v[96:99]\n"
    "ds_read_b128 a[8:11], v200 offset:32768\n"
    "v_mfma_f32_16x16x32_bf16 v[100:103], a[56:59], a[36:39], v[100:103]\n"
    "ds_read_b64_tr_b16 a[24:25], v50 offset:49152\n"
    "s_add_u32 m0, s36, 16384\n"
    "s_bfe_u32 s38, s57, 0x40018\n"
    "s_lshl_b32 s38, s38, 19\n"
    "buffer_load_dwordx4 v220, s[12:15], s38 offen lds\n"
    "buffer_load_dwordx4 v221, s[12:15], s38 offen offset:1024 lds\n"
    "v_mfma_f32_16x16x32_bf16 v[104:107], a[56:59], a[40:43], v[104:107]\n"
    "ds_read_b64_tr_b16 a[26:27], v50 offset:49664\n"
    "s_waitcnt lgkmcnt(11)\n"
    "v_mfma_f32_16x16x32_bf16 v[76:79], a[48:51], a[44:47], v[76:79]\n"
    "ds_read_b128 a[12:15], v200 offset:34816\n"
    "buffer_load_dwordx4 v222, s[12:15], s38 offen offset:2048 lds\n"
    "v_mfma_f32_16x16x32_bf16 v[92:95], a[52:55], a[44:47], v[92:95]\n"
    "ds_read_b64_tr_b16 a[28:29], v51 offset:49152\n"
    "v_mfma_f32_16x16x32_bf16 v[108:111], a[56:59], a[44:47], v[108:111]\n"
    "ds_read_b64_tr_b16 a[30:31], v51 offset:49664\n"
    "buffer_load_dwordx4 v223, s[12:15], s38 offen offset:3072 lds\n"
    "s_waitcnt lgkmcnt(12)\n"
    "v_mfma_f32_16x16x32_bf16 v[112:115], a[60:63], a[32:35], v[112:115]\n"
    "v_mfma_f32_16x16x32_bf16 v[116:119], a[60:63], a[36:39], v[116:119]\n"
    "v_mfma_f32_16x16x32_bf16 v[120:123], a[60:63], a[40:43], v[120:123]\n"
    "v_mfma_f32_16x16x32_bf16 v[124:127], a[60:63], a[44:47], v[124:127]\n"
    "s_add_u32 m0, s36, 32768\n"
    "s_bfe_u32 s38, s57, 0x4001c\n"
    "s_lshl_b32 s38, s38, 7\n"
    "buffer_load_dwordx4 v216, s[8:11], s38 offen lds\n"
    "; ================ period 12 ================\n"
    "s_waitcnt lgkmcnt(3)\n"
    "v_mfma_f32_16x16x32_bf16 v[64:67], a[16:19], a[0:3], v[64:67]\n"
    "ds_read_b128 a[32:35], v195 offset:32768\n"
    "v_mfma_f32_16x16x32_bf16 v[68:71], a[16:19], a[4:7], v[68:71]\n"
    "ds_read_b64_tr_b16 a[48:49], v48 offset:53248\n"
    "v_mfma_f32_16x16x32_bf16 v[80:83], a[20:23], a[0:3], v[80:83]\n"
    "ds_read_b64_tr_b16 a[50:51], v48 offset:53760\n"
    "v_mfma_f32_16x16x32_bf16 v[84:87], a[20:23], a[4:7], v[84:87]\n"
    "ds_read_b128 a[36:39], v195 offset:34816\n"
    "buffer_load_dwordx4 v217, s[8:11], s38 offen offset:1024 lds\n"
    "v_mfma_f32_16x16x32_bf16 v[72:75], a[16:19], a[8:11], v[72:75]\n"
    "ds_read_b64_tr_b16 a[52:53], v49 offset:53248\n"
    "v_mfma_f32_16x16x32_bf16 v[88:91], a[20:23], a[8:11], v[88:91]\n"
    "ds_read_b64_tr_b16 a[54:55], v49 offset:53760\n"
    "v_mfma_f32_16x16x32_bf16 v[96:99], a[24:27], a[0:3], v[96:99]\n"
    "ds_read_b128 a[40:43], v201 offset:32768\n"
    "v_mfma_f32_16x16x32_bf16 v[100:103], a[24:27], a[4:7], v[100:103]\n"
    "ds_read_b64_tr_b16 a[56:57], v50 offset:53248\n"
    "buffer_load_dwordx4 v218, s[8:11], s38 offen offset:2048 lds\n"
    "v_mfma_f32_16x16x32_bf16 v[104:107], a[24:27], a[8:11], v[104:107]\n"
    "ds_read_b64_tr_b16 a[58:59], v50 offset:53760\n"
    "s_waitcnt lgkmcnt(11)\n"
    "v_mfma_f32_16x16x32_bf16 v[76:79], a[16:19], a[12:15], v[76:79]\n"
    "ds_read_b128 a[44:47], v201 offset:34816\n"
    "v_mfma_f32_16x16x32_bf16 v[92:95], a[20:23], a[12:15], v[92:95]\n"
    "ds_read_b64_tr_b16 a[60:61], v51 offset:53248\n"
    "v_mfma_f32_16x16x32_bf16 v[108:111], a[24:27], a[12:15], v[108:111]\n"
    "ds_read_b64_tr_b16 a[62:63], v51 offset:53760\n"
    "buffer_load_dwordx4 v219, s[8:11], s38 offen offset:3072 lds\n"
    "s_waitcnt lgkmcnt(12)\n"
    "v_mfma_f32_16x16x32_bf16 v[112:115], a[28:31], a[0:3], v[112:115]\n"
    "v_mfma_f32_16x16x32_bf16 v[116:119], a[28:31], a[4:7], v[116:119]\n"
    "v_mfma_f32_16x16x32_bf16 v[120:123], a[28:31], a[8:11], v[120:123]\n"
    "v_mfma_f32_16x16x32_bf16 v[124:127], a[28:31], a[12:15], v[124:127]\n"
    "s_add_u32 m0, s36, 49152\n"
    "s_bfe_u32 s38, s57, 0x4001c\n"
    "s_lshl_b32 s38, s38, 19\n"
    "buffer_load_dwordx4 v220, s[12:15], s38 offen lds\n"
    "s_waitcnt vmcnt(13)\n"
    "s_barrier\n"
    "s_waitcnt lgkmcnt(3)\n"
    "v_mfma_f32_16x16x32_bf16 v[64:67], a[48:51], a[32:35], v[64:67]\n"
    "ds_read_b128 a[0:3], v196 offset:0\n"
    "v_mfma_f32_16x16x32_bf16 v[68:71], a[48:51], a[36:39], v[68:71]\n"
    "ds_read_b64_tr_b16 a[16:17], v52 offset:16384\n"
    "buffer_load_dwordx4 v221, s[12:15], s38 offen offset:1024 lds\n"
    "v_mfma_f32_16x16x32_bf16 v[80:83], a[52:55], a[32:35], v[80:83]\n"
    "ds_read_b64_tr_b16 a[18:19], v52 offset:16896\n"
    "v_mfma_f32_16x16x32_bf16 v[84:87], a[52:55], a[36:39], v[84:87]\n"
    "ds_read_b128 a[4:7], v196 offset:2048\n"
    "buffer_load_dwordx4 v222, s[12:15], s38 offen offset:2048 lds\n"
    "buffer_load_dwordx4 v223, s[12:15], s38 offen offset:3072 lds\n"
    "v_mfma_f32_16x16x32_bf16 v[72:75], a[48:51], a[40:43], v[72:75]\n"
    "ds_read_b64_tr_b16 a[20:21], v53 offset:16384\n"
    "v_mfma_f32_16x16x32_bf16 v[88:91], a[52:55], a[40:43], v[88:91]\n"
    "ds_read_b64_tr_b16 a[22:23], v53 offset:16896\n"
    "v_mfma_f32_16x16x32_bf16 v[96:99], a[56:59], a[32:35], v[96:99]\n"
    "ds_read_b128 a[8:11], v202 offset:0\n"
    "v_mfma_f32_16x16x32_bf16 v[100:103], a[56:59], a[36:39], v[100:103]\n"
    "ds_read_b64_tr_b16 a[24:25], v54 offset:16384\n"
    "s_add_u32 m0, s36, 65536\n"
    "s_bfe_u32 s38, s58, 0x40000\n"
    "s_lshl_b32 s38, s38, 13\n"
    "buffer_load_dwordx4 v12, s[20:23], s38 offen lds\n"
    "v_mfma_f32_16x16x32_bf16 v[104:107], a[56:59], a[40:43], v[104:107]\n"
    "ds_read_b64_tr_b16 a[26:27], v54 offset:16896\n"
    "s_waitcnt lgkmcnt(11)\n"
    "v_mfma_f32_16x16x32_bf16 v[76:79], a[48:51], a[44:47], v[76:79]\n"
    "ds_read_b128 a[12:15], v202 offset:2048\n"
    "v_mfma_f32_16x16x32_bf16 v[92:95], a[52:55], a[44:47], v[92:95]\n"
    "ds_read_b64_tr_b16 a[28:29], v55 offset:16384\n"
    "v_mfma_f32_16x16x32_bf16 v[108:111], a[56:59], a[44:47], v[108:111]\n"
    "ds_read_b64_tr_b16 a[30:31], v55 offset:16896\n"
    "buffer_load_dwordx4 v13, s[20:23], s38 offen offset:1024 lds\n"
    "s_waitcnt lgkmcnt(12)\n"
    "v_mfma_f32_16x16x32_bf16 v[112:115], a[60:63], a[32:35], v[112:115]\n"
    "v_mfma_f32_16x16x32_bf16 v[116:119], a[60:63], a[36:39], v[116:119]\n"
    "v_mfma_f32_16x16x32_bf16 v[120:123], a[60:63], a[40:43], v[120:123]\n"
    "v_mfma_f32_16x16x32_bf16 v[124:127], a[60:63], a[44:47], v[124:127]\n"
    "buffer_load_dwordx4 v14, s[20:23], s38 offen offset:2048 lds\n"
    "; ================ period 13 ================\n"
    "s_waitcnt lgkmcnt(3)\n"
    "v_mfma_f32_16x16x32_bf16 v[64:67], a[16:19], a[0:3], v[64:67]\n"
    "ds_read_b128 a[32:35], v197 offset:0\n"
    "v_mfma_f32_16x16x32_bf16 v[68:71], a[16:19], a[4:7], v[68:71]\n"
    "ds_read_b64_tr_b16 a[48:49], v52 offset:20480\n"
    "v_mfma_f32_16x16x32_bf16 v[80:83], a[20:23], a[0:3], v[80:83]\n"
    "ds_read_b64_tr_b16 a[50:51], v52 offset:20992\n"
    "v_mfma_f32_16x16x32_bf16 v[84:87], a[20:23], a[4:7], v[84:87]\n"
    "ds_read_b128 a[36:39], v197 offset:2048\n"
    "buffer_load_dwordx4 v15, s[20:23], s38 offen offset:3072 lds\n"
    "v_mfma_f32_16x16x32_bf16 v[72:75], a[16:19], a[8:11], v[72:75]\n"
    "ds_read_b64_tr_b16 a[52:53], v53 offset:20480\n"
    "v_mfma_f32_16x16x32_bf16 v[88:91], a[20:23], a[8:11], v[88:91]\n"
    "ds_read_b64_tr_b16 a[54:55], v53 offset:20992\n"
    "v_mfma_f32_16x16x32_bf16 v[96:99], a[24:27], a[0:3], v[96:99]\n"
    "ds_read_b128 a[40:43], v203 offset:0\n"
    "v_mfma_f32_16x16x32_bf16 v[100:103], a[24:27], a[4:7], v[100:103]\n"
    "ds_read_b64_tr_b16 a[56:57], v54 offset:20480\n"
    "s_add_u32 m0, s36, 81920\n"
    "s_bfe_u32 s38, s58, 0x40000\n"
    "s_lshl_b32 s38, s38, 18\n"
    "buffer_load_dwordx4 v16, s[24:27], s38 offen lds\n"
    "v_mfma_f32_16x16x32_bf16 v[104:107], a[24:27], a[8:11], v[104:107]\n"
    "ds_read_b64_tr_b16 a[58:59], v54 offset:20992\n"
    "s_waitcnt lgkmcnt(11)\n"
    "v_mfma_f32_16x16x32_bf16 v[76:79], a[16:19], a[12:15], v[76:79]\n"
    "ds_read_b128 a[44:47], v203 offset:2048\n"
    "v_mfma_f32_16x16x32_bf16 v[92:95], a[20:23], a[12:15], v[92:95]\n"
    "ds_read_b64_tr_b16 a[60:61], v55 offset:20480\n"
    "v_mfma_f32_16x16x32_bf16 v[108:111], a[24:27], a[12:15], v[108:111]\n"
    "ds_read_b64_tr_b16 a[62:63], v55 offset:20992\n"
    "buffer_load_dwordx4 v17, s[24:27], s38 offen offset:1024 lds\n"
    "s_waitcnt lgkmcnt(12)\n"
    "v_mfma_f32_16x16x32_bf16 v[112:115], a[28:31], a[0:3], v[112:115]\n"
    "v_mfma_f32_16x16x32_bf16 v[116:119], a[28:31], a[4:7], v[116:119]\n"
    "v_mfma_f32_16x16x32_bf16 v[120:123], a[28:31], a[8:11], v[120:123]\n"
    "v_mfma_f32_16x16x32_bf16 v[124:127], a[28:31], a[12:15], v[124:127]\n"
    "buffer_load_dwordx4 v18, s[24:27], s38 offen offset:2048 lds\n"
    "s_waitcnt vmcnt(15)\n"
    "s_barrier\n"
    "s_waitcnt lgkmcnt(3)\n"
    "v_mfma_f32_16x16x32_bf16 v[64:67], a[48:51], a[32:35], v[64:67]\n"
    "ds_read_b128 a[0:3], v192 offset:0\n"
    "v_mfma_f32_16x16x32_bf16 v[68:71], a[48:51], a[36:39], v[68:71]\n"
    "ds_read_b64_tr_b16 a[16:17], v6 offset:16384\n"
    "buffer_load_dwordx4 v19, s[24:27], s38 offen offset:3072 lds\n"
    "v_mfma_f32_16x16x32_bf16 v[80:83], a[52:55], a[32:35], v[80:83]\n"
    "ds_read_b64_tr_b16 a[18:19], v6 offset:16896\n"
    "v_mfma_f32_16x16x32_bf16 v[84:87], a[52:55], a[36:39], v[84:87]\n"
    "ds_read_b128 a[4:7], v192 offset:2048\n"
    "s_add_u32 m0, s36, 98304\n"
    "s_bfe_u32 s38, s58, 0x40000\n"
    "s_lshl_b32 s38, s38, 13\n"
    "s_or_b32 s38, s38, 128\n"
    "buffer_load_dwordx4 v12, s[20:23], s38 offen lds\n"
    "v_mfma_f32_16x16x32_bf16 v[72:75], a[48:51], a[40:43], v[72:75]\n"
    "ds_read_b64_tr_b16 a[20:21], v7 offset:16384\n"
    "v_mfma_f32_16x16x32_bf16 v[88:91], a[52:55], a[40:43], v[88:91]\n"
    "ds_read_b64_tr_b16 a[22:23], v7 offset:16896\n"
    "v_mfma_f32_16x16x32_bf16 v[96:99], a[56:59], a[32:35], v[96:99]\n"
    "ds_read_b128 a[8:11], v198 offset:0\n"
    "v_mfma_f32_16x16x32_bf16 v[100:103], a[56:59], a[36:39], v[100:103]\n"
    "ds_read_b64_tr_b16 a[24:25], v8 offset:16384\n"
    "buffer_load_dwordx4 v13, s[20:23], s38 offen offset:1024 lds\n"
    "v_mfma_f32_16x16x32_bf16 v[104:107], a[56:59], a[40:43], v[104:107]\n"
    "ds_read_b64_tr_b16 a[26:27], v8 offset:16896\n"
    "s_waitcnt lgkmcnt(11)\n"
    "v_mfma_f32_16x16x32_bf16 v[76:79], a[48:51], a[44:47], v[76:79]\n"
    "ds_read_b128 a[12:15], v198 offset:2048\n"
    "v_mfma_f32_16x16x32_bf16 v[92:95], a[52:55], a[44:47], v[92:95]\n"
    "ds_read_b64_tr_b16 a[28:29], v9 offset:16384\n"
    "v_mfma_f32_16x16x32_bf16 v[108:111], a[56:59], a[44:47], v[108:111]\n"
    "ds_read_b64_tr_b16 a[30:31], v9 offset:16896\n"
    "buffer_load_dwordx4 v14, s[20:23], s38 offen offset:2048 lds\n"
    "s_waitcnt lgkmcnt(12)\n"
    "v_mfma_f32_16x16x32_bf16 v[112:115], a[60:63], a[32:35], v[112:115]\n"
    "v_mfma_f32_16x16x32_bf16 v[116:119], a[60:63], a[36:39], v[116:119]\n"
    "v_mfma_f32_16x16x32_bf16 v[120:123], a[60:63], a[40:43], v[120:123]\n"
    "v_mfma_f32_16x16x32_bf16 v[124:127], a[60:63], a[44:47], v[124:127]\n"
    "buffer_load_dwordx4 v15, s[20:23], s38 offen offset:3072 lds\n"
    "; ================ period 14 ================\n"
    "s_waitcnt lgkmcnt(3)\n"
    "v_mfma_f32_16x16x32_bf16 v[64:67], a[16:19], a[0:3], v[64:67]\n"
    "ds_read_b128 a[32:35], v193 offset:0\n"
    "v_mfma_f32_16x16x32_bf16 v[68:71], a[16:19], a[4:7], v[68:71]\n"
    "ds_read_b64_tr_b16 a[48:49], v6 offset:20480\n"
    "v_add_co_u32_e64 v32, s[50:51], 1, v32\n"
    "v_mfma_f32_16x16x32_bf16 v[80:83], a[20:23], a[0:3], v[80:83]\n"
    "ds_read_b64_tr_b16 a[50:51], v6 offset:20992\n"
    "v_mfma_f32_16x16x32_bf16 v[84:87], a[20:23], a[4:7], v[84:87]\n"
    "ds_read_b128 a[36:39], v193 offset:2048\n"
    "s_add_u32 m0, s36, 114688\n"
    "s_bfe_u32 s38, s58, 0x40000\n"
    "s_lshl_b32 s38, s38, 18\n"
    "s_or_b32 s38, s38, 131072\n"
    "buffer_load_dwordx4 v16, s[24:27], s38 offen lds\n"
    "s_nop 1\n"
    "v_addc_co_u32_e64 v33, s[50:51], 0, v33, s[50:51]\n"
    "v_mfma_f32_16x16x32_bf16 v[72:75], a[16:19], a[8:11], v[72:75]\n"
    "ds_read_b64_tr_b16 a[52:53], v7 offset:20480\n"
    "v_mfma_f32_16x16x32_bf16 v[88:91], a[20:23], a[8:11], v[88:91]\n"
    "ds_read_b64_tr_b16 a[54:55], v7 offset:20992\n"
    "v_add_co_u32_e64 v34, s[50:51], 1, v34\n"
    "v_mfma_f32_16x16x32_bf16 v[96:99], a[24:27], a[0:3], v[96:99]\n"
    "ds_read_b128 a[40:43], v199 offset:0\n"
    "v_mfma_f32_16x16x32_bf16 v[100:103], a[24:27], a[4:7], v[100:103]\n"
    "ds_read_b64_tr_b16 a[56:57], v8 offset:20480\n"
    "buffer_load_dwordx4 v17, s[24:27], s38 offen offset:1024 lds\n"
    "s_nop 1\n"
    "v_addc_co_u32_e64 v35, s[50:51], 0, v35, s[50:51]\n"
    "v_mfma_f32_16x16x32_bf16 v[104:107], a[24:27], a[8:11], v[104:107]\n"
    "ds_read_b64_tr_b16 a[58:59], v8 offset:20992\n"
    "s_waitcnt lgkmcnt(11)\n"
    "v_mfma_f32_16x16x32_bf16 v[76:79], a[16:19], a[12:15], v[76:79]\n"
    "ds_read_b128 a[44:47], v199 offset:2048\n"
    "v_mfma_f32_16x16x32_bf16 v[92:95], a[20:23], a[12:15], v[92:95]\n"
    "ds_read_b64_tr_b16 a[60:61], v9 offset:20480\n"
    "v_mfma_f32_16x16x32_bf16 v[108:111], a[24:27], a[12:15], v[108:111]\n"
    "ds_read_b64_tr_b16 a[62:63], v9 offset:20992\n"
    "buffer_load_dwordx4 v18, s[24:27], s38 offen offset:2048 lds\n"
    "s_waitcnt lgkmcnt(12)\n"
    "v_mfma_f32_16x16x32_bf16 v[112:115], a[28:31], a[0:3], v[112:115]\n"
    "v_mfma_f32_16x16x32_bf16 v[116:119], a[28:31], a[4:7], v[116:119]\n"
    "v_mfma_f32_16x16x32_bf16 v[120:123], a[28:31], a[8:11], v[120:123]\n"
    "v_mfma_f32_16x16x32_bf16 v[124:127], a[28:31], a[12:15], v[124:127]\n"
    "buffer_load_dwordx4 v19, s[24:27], s38 offen offset:3072 lds\n"
    "s_waitcnt vmcnt(16)\n"
    "s_barrier\n"
    "s_waitcnt lgkmcnt(3)\n"
    "v_mfma_f32_16x16x32_bf16 v[64:67], a[48:51], a[32:35], v[64:67]\n"
    "ds_read_b128 a[0:3], v192 offset:32768\n"
    "v_mfma_f32_16x16x32_bf16 v[68:71], a[48:51], a[36:39], v[68:71]\n"
    "ds_read_b64_tr_b16 a[16:17], v6 offset:49152\n"
    "v_mfma_f32_16x16x32_bf16 v[80:83], a[52:55], a[32:35], v[80:83]\n"
    "ds_read_b64_tr_b16 a[18:19], v6 offset:49664\n"
    "v_mfma_f32_16x16x32_bf16 v[84:87], a[52:55], a[36:39], v[84:87]\n"
    "ds_read_b128 a[4:7], v192 offset:34816\n"
    "s_add_u32 m0, s36, 131072\n"
    "s_bfe_u32 s38, s58, 0x40004\n"
    "s_lshl_b32 s38, s38, 13\n"
    "buffer_load_dwordx4 v12, s[20:23], s38 offen lds\n"
    "v_mfma_f32_16x16x32_bf16 v[72:75], a[48:51], a[40:43], v[72:75]\n"
    "ds_read_b64_tr_b16 a[20:21], v7 offset:49152\n"
    "v_mfma_f32_16x16x32_bf16 v[88:91], a[52:55], a[40:43], v[88:91]\n"
    "ds_read_b64_tr_b16 a[22:23], v7 offset:49664\n"
    "v_mfma_f32_16x16x32_bf16 v[96:99], a[56:59], a[32:35], v[96:99]\n"
    "ds_read_b128 a[8:11], v198 offset:32768\n"
    "v_mfma_f32_16x16x32_bf16 v[100:103], a[56:59], a[36:39], v[100:103]\n"
    "ds_read_b64_tr_b16 a[24:25], v8 offset:49152\n"
    "buffer_load_dwordx4 v13, s[20:23], s38 offen offset:1024 lds\n"
    "v_mfma_f32_16x16x32_bf16 v[104:107], a[56:59], a[40:43], v[104:107]\n"
    "ds_read_b64_tr_b16 a[26:27], v8 offset:49664\n"
    "s_waitcnt lgkmcnt(11)\n"
    "v_mfma_f32_16x16x32_bf16 v[76:79], a[48:51], a[44:47], v[76:79]\n"
    "ds_read_b128 a[12:15], v198 offset:34816\n"
    "v_mfma_f32_16x16x32_bf16 v[92:95], a[52:55], a[44:47], v[92:95]\n"
    "ds_read_b64_tr_b16 a[28:29], v9 offset:49152\n"
    "v_mfma_f32_16x16x32_bf16 v[108:111], a[56:59], a[44:47], v[108:111]\n"
    "ds_read_b64_tr_b16 a[30:31], v9 offset:49664\n"
    "buffer_load_dwordx4 v14, s[20:23], s38 offen offset:2048 lds\n"
    "s_waitcnt lgkmcnt(12)\n"
    "v_mfma_f32_16x16x32_bf16 v[112:115], a[60:63], a[32:35], v[112:115]\n"
    "v_mfma_f32_16x16x32_bf16 v[116:119], a[60:63], a[36:39], v[116:119]\n"
    "v_mfma_f32_16x16x32_bf16 v[120:123], a[60:63], a[40:43], v[120:123]\n"
    "v_mfma_f32_16x16x32_bf16 v[124:127], a[60:63], a[44:47], v[124:127]\n"
    "buffer_load_dwordx4 v15, s[20:23], s38 offen offset:3072 lds\n"
    "; ================ period 15 ================\n"
    "s_waitcnt lgkmcnt(3)\n"
    "v_mfma_f32_16x16x32_bf16 v[64:67], a[16:19], a[0:3], v[64:67]\n"
    "ds_read_b128 a[32:35], v193 offset:32768\n"
    "v_mfma_f32_16x16x32_bf16 v[68:71], a[16:19], a[4:7], v[68:71]\n"
    "ds_read_b64_tr_b16 a[48:49], v6 offset:53248\n"
    "v_mfma_f32_16x16x32_bf16 v[80:83], a[20:23], a[0:3], v[80:83]\n"
    "ds_read_b64_tr_b16 a[50:51], v6 offset:53760\n"
    "v_mfma_f32_16x16x32_bf16 v[84:87], a[20:23], a[4:7], v[84:87]\n"
    "ds_read_b128 a[36:39], v193 offset:34816\n"
    "s_add_u32 m0, s36, 147456\n"
    "s_bfe_u32 s38, s58, 0x40004\n"
    "s_lshl_b32 s38, s38, 18\n"
    "buffer_load_dwordx4 v16, s[24:27], s38 offen lds\n"
    "v_mfma_f32_16x16x32_bf16 v[72:75], a[16:19], a[8:11], v[72:75]\n"
    "ds_read_b64_tr_b16 a[52:53], v7 offset:53248\n"
    "v_mfma_f32_16x16x32_bf16 v[88:91], a[20:23], a[8:11], v[88:91]\n"
    "ds_read_b64_tr_b16 a[54:55], v7 offset:53760\n"
    "v_mfma_f32_16x16x32_bf16 v[96:99], a[24:27], a[0:3], v[96:99]\n"
    "ds_read_b128 a[40:43], v199 offset:32768\n"
    "v_mfma_f32_16x16x32_bf16 v[100:103], a[24:27], a[4:7], v[100:103]\n"
    "ds_read_b64_tr_b16 a[56:57], v8 offset:53248\n"
    "buffer_load_dwordx4 v17, s[24:27], s38 offen offset:1024 lds\n"
    "v_mfma_f32_16x16x32_bf16 v[104:107], a[24:27], a[8:11], v[104:107]\n"
    "ds_read_b64_tr_b16 a[58:59], v8 offset:53760\n"
    "s_waitcnt lgkmcnt(11)\n"
    "v_mfma_f32_16x16x32_bf16 v[76:79], a[16:19], a[12:15], v[76:79]\n"
    "ds_read_b128 a[44:47], v199 offset:34816\n"
    "v_mfma_f32_16x16x32_bf16 v[92:95], a[20:23], a[12:15], v[92:95]\n"
    "ds_read_b64_tr_b16 a[60:61], v9 offset:53248\n"
    "v_mfma_f32_16x16x32_bf16 v[108:111], a[24:27], a[12:15], v[108:111]\n"
    "ds_read_b64_tr_b16 a[62:63], v9 offset:53760\n"
    "buffer_load_dwordx4 v18, s[24:27], s38 offen offset:2048 lds\n"
    "s_waitcnt lgkmcnt(12)\n"
    "v_mfma_f32_16x16x32_bf16 v[112:115], a[28:31], a[0:3], v[112:115]\n"
    "v_mfma_f32_16x16x32_bf16 v[116:119], a[28:31], a[4:7], v[116:119]\n"
    "v_mfma_f32_16x16x32_bf16 v[120:123], a[28:31], a[8:11], v[120:123]\n"
    "v_mfma_f32_16x16x32_bf16 v[124:127], a[28:31], a[12:15], v[124:127]\n"
    "buffer_load_dwordx4 v19, s[24:27], s38 offen offset:3072 lds\n"
    "s_waitcnt vmcnt(16) lgkmcnt(0)\n"
    "s_barrier\n"
    "v_mfma_f32_16x16x32_bf16 v[64:67], a[48:51], a[32:35], v[64:67]\n"
    "ds_read_b128 a[0:3], v44 offset:0\n"
    "v_mfma_f32_16x16x32_bf16 v[68:71], a[48:51], a[36:39], v[68:71]\n"
    "ds_read_b64_tr_b16 a[16:17], v48 offset:16384\n"
    "s_add_u32 m0, s36, 0\n"
    "s_bfe_u32 s38, s58, 0x40004\n"
    "s_lshl_b32 s38, s38, 13\n"
    "s_or_b32 s38, s38, 128\n"
    "buffer_load_dwordx4 v12, s[20:23], s38 offen lds\n"
    "v_mfma_f32_16x16x32_bf16 v[80:83], a[52:55], a[32:35], v[80:83]\n"
    "ds_read_b64_tr_b16 a[18:19], v48 offset:16896\n"
    "v_mfma_f32_16x16x32_bf16 v[84:87], a[52:55], a[36:39], v[84:87]\n"
    "ds_read_b128 a[4:7], v44 offset:2048\n"
    "v_mfma_f32_16x16x32_bf16 v[72:75], a[48:51], a[40:43], v[72:75]\n"
    "ds_read_b64_tr_b16 a[20:21], v49 offset:16384\n"
    "v_mfma_f32_16x16x32_bf16 v[88:91], a[52:55], a[40:43], v[88:91]\n"
    "ds_read_b64_tr_b16 a[22:23], v49 offset:16896\n"
    "v_mfma_f32_16x16x32_bf16 v[96:99], a[56:59], a[32:35], v[96:99]\n"
    "ds_read_b128 a[8:11], v44 offset:4096\n"
    "v_mfma_f32_16x16x32_bf16 v[100:103], a[56:59], a[36:39], v[100:103]\n"
    "ds_read_b64_tr_b16 a[24:25], v50 offset:16384\n"
    "v_mfma_f32_16x16x32_bf16 v[104:107], a[56:59], a[40:43], v[104:107]\n"
    "ds_read_b64_tr_b16 a[26:27], v50 offset:16896\n"
    "v_mfma_f32_16x16x32_bf16 v[76:79], a[48:51], a[44:47], v[76:79]\n"
    "ds_read_b128 a[12:15], v44 offset:6144\n"
    "v_mfma_f32_16x16x32_bf16 v[92:95], a[52:55], a[44:47], v[92:95]\n"
    "ds_read_b64_tr_b16 a[28:29], v51 offset:16384\n"
    "v_mfma_f32_16x16x32_bf16 v[108:111], a[56:59], a[44:47], v[108:111]\n"
    "ds_read_b64_tr_b16 a[30:31], v51 offset:16896\n"
    "v_mfma_f32_16x16x32_bf16 v[112:115], a[60:63], a[32:35], v[112:115]\n"
    "v_mfma_f32_16x16x32_bf16 v[116:119], a[60:63], a[36:39], v[116:119]\n"
    "v_mfma_f32_16x16x32_bf16 v[120:123], a[60:63], a[40:43], v[120:123]\n"
    "v_mfma_f32_16x16x32_bf16 v[124:127], a[60:63], a[44:47], v[124:127]\n"
    "; ================ period 16 ================\n"
    "s_waitcnt lgkmcnt(3)\n"
    "v_mfma_f32_16x16x32_bf16 v[128:131], a[0:3], a[16:19], 0\n"
    "ds_read_b128 a[32:35], v45 offset:0\n"
    "v_cvt_pk_bf16_f32 v200, v72, v73\n"
    "v_cvt_pk_bf16_f32 v201, v74, v75\n"
    "v_cvt_pk_bf16_f32 v202, v88, v89\n"
    "v_cvt_pk_bf16_f32 v203, v90, v91\n"
    "v_mfma_f32_16x16x32_bf16 v[144:147], a[4:7], a[16:19], 0\n"
    "ds_read_b64_tr_b16 a[48:49], v48 offset:20480\n"
    "v_cvt_pk_bf16_f32 v216, v104, v105\n"
    "v_cvt_pk_bf16_f32 v217, v106, v107\n"
    "v_cvt_pk_bf16_f32 v218, v120, v121\n"
    "v_cvt_pk_bf16_f32 v219, v122, v123\n"
    "v_mfma_f32_16x16x32_bf16 v[132:135], a[0:3], a[20:23], 0\n"
    "ds_read_b64_tr_b16 a[50:51], v48 offset:20992\n"
    "v_cvt_pk_bf16_f32 v204, v76, v77\n"
    "v_cvt_pk_bf16_f32 v205, v78, v79\n"
    "v_cvt_pk_bf16_f32 v206, v92, v93\n"
    "v_cvt_pk_bf16_f32 v207, v94, v95\n"
    "v_mfma_f32_16x16x32_bf16 v[148:151], a[4:7], a[20:23], 0\n"
    "ds_read_b128 a[36:39], v45 offset:2048\n"
    "v_cvt_pk_bf16_f32 v220, v108, v109\n"
    "v_cvt_pk_bf16_f32 v221, v110, v111\n"
    "v_cvt_pk_bf16_f32 v222, v124, v125\n"
    "v_cvt_pk_bf16_f32 v223, v126, v127\n"
    "v_mfma_f32_16x16x32_bf16 v[160:163], a[8:11], a[16:19], 0\n"
    "ds_read_b64_tr_b16 a[52:53], v49 offset:20480\n"
    "v_cvt_pk_bf16_f32 v192, v64, v65\n"
    "v_cvt_pk_bf16_f32 v193, v66, v67\n"
    "v_cvt_pk_bf16_f32 v194, v80, v81\n"
    "v_cvt_pk_bf16_f32 v195, v82, v83\n"
    "v_mfma_f32_16x16x32_bf16 v[240:243], a[240:243], v[200:203], 0\n"
    "v_mfma_f32_16x16x32_bf16 v[164:167], a[8:11], a[20:23], 0\n"
    "ds_read_b64_tr_b16 a[54:55], v49 offset:20992\n"
    "v_cvt_pk_bf16_f32 v208, v96, v97\n"
    "v_cvt_pk_bf16_f32 v209, v98, v99\n"
    "v_cvt_pk_bf16_f32 v210, v112, v113\n"
    "v_cvt_pk_bf16_f32 v211, v114, v115\n"
    "v_mfma_f32_16x16x32_bf16 v[240:243], a[244:247], v[216:219], v[240:243]\n"
    "v_mfma_f32_16x16x32_bf16 v[136:139], a[0:3], a[24:27], 0\n"
    "ds_read_b128 a[40:43], v45 offset:4096\n"
    "v_cvt_pk_bf16_f32 v196, v68, v69\n"
    "v_cvt_pk_bf16_f32 v197, v70, v71\n"
    "v_cvt_pk_bf16_f32 v198, v84, v85\n"
    "v_cvt_pk_bf16_f32 v199, v86, v87\n"
    "v_mfma_f32_16x16x32_bf16 v[244:247], a[240:243], v[204:207], 0\n"
    "v_mfma_f32_16x16x32_bf16 v[152:155], a[4:7], a[24:27], 0\n"
    "ds_read_b64_tr_b16 a[56:57], v50 offset:20480\n"
    "v_cvt_pk_bf16_f32 v212, v100, v101\n"
    "v_cvt_pk_bf16_f32 v213, v102, v103\n"
    "v_cvt_pk_bf16_f32 v214, v116, v117\n"
    "v_cvt_pk_bf16_f32 v215, v118, v119\n"
    "v_mfma_f32_16x16x32_bf16 v[244:247], a[244:247], v[220:223], v[244:247]\n"
    "v_mfma_f32_16x16x32_bf16 v[168:171], a[8:11], a[24:27], 0\n"
    "ds_read_b64_tr_b16 a[58:59], v50 offset:20992\n"
    "v_mfma_f32_16x16x32_bf16 v[248:251], a[248:251], v[200:203], 0\n"
    "s_waitcnt lgkmcnt(11)\n"
    "v_mfma_f32_16x16x32_bf16 v[176:179], a[12:15], a[16:19], 0\n"
    "ds_read_b128 a[44:47], v45 offset:6144\n"
    "v_mfma_f32_16x16x32_bf16 v[248:251], a[252:255], v[216:219], v[248:251]\n"
    "v_mfma_f32_16x16x32_bf16 v[180:183], a[12:15], a[20:23], 0\n"
    "ds_read_b64_tr_b16 a[60:61], v51 offset:20480\n"
    "v_mfma_f32_16x16x32_bf16 v[252:255], a[248:251], v[204:207], 0\n"
    "v_mfma_f32_16x16x32_bf16 v[184:187], a[12:15], a[24:27], 0\n"
    "ds_read_b64_tr_b16 a[62:63], v51 offset:20992\n"
    "v_mfma_f32_16x16x32_bf16 v[252:255], a[252:255], v[220:223], v[252:255]\n"
    "s_waitcnt lgkmcnt(12)\n"
    "v_mfma_f32_16x16x32_bf16 v[140:143], a[0:3], a[28:31], 0\n"
    "ds_write_b128 v11, v[240:243] offset:0\n"
    "v_mfma_f32_16x16x32_bf16 v[156:159], a[4:7], a[28:31], 0\n"
    "ds_write_b128 v11, v[244:247] offset:1024\n"
    "v_mfma_f32_16x16x32_bf16 v[172:175], a[8:11], a[28:31], 0\n"
    "ds_write_b128 v11, v[248:251] offset:2048\n"
    "v_mfma_f32_16x16x32_bf16 v[188:191], a[12:15], a[28:31], 0\n"
    "s_waitcnt lgkmcnt(8)\n"
    "ds_write_b128 v11, v[252:255] offset:3072\n"
    "s_waitcnt vmcnt(9) lgkmcnt(0)\n"
    "s_barrier\n"
    "v_mfma_f32_16x16x32_bf16 v[128:131], a[32:35], a[48:51], v[128:131]\n"
    "ds_read_b128 a[0:3], v44 offset:32768\n"
    "v_mfma_f32_16x16x32_bf16 v[224:227], a[224:227], v[192:195], 0\n"
    "v_mfma_f32_16x16x32_bf16 v[144:147], a[36:39], a[48:51], v[144:147]\n"
    "ds_read_b64_tr_b16 a[16:17], v48 offset:49152\n"
    "buffer_load_dwordx4 v13, s[20:23], s38 offen offset:1024 lds\n"
    "v_mfma_f32_16x16x32_bf16 v[224:227], a[228:231], v[208:211], v[224:227]\n"
    "ds_read_b128 v[240:243], v43 offset:0\n"
    "v_mfma_f32_16x16x32_bf16 v[132:135], a[32:35], a[52:55], v[132:135]\n"
    "ds_read_b64_tr_b16 a[18:19], v48 offset:49664\n"
    "v_mfma_f32_16x16x32_bf16 v[228:231], a[224:227], v[196:199], 0\n"
    "ds_read_b128 v[244:247], v43 offset:1024\n"
    "v_mfma_f32_16x16x32_bf16 v[148:151], a[36:39], a[52:55], v[148:151]\n"
    "ds_read_b128 a[4:7], v44 offset:34816\n"
    "buffer_load_dwordx4 v14, s[20:23], s38 offen offset:2048 lds\n"
    "buffer_load_dwordx4 v15, s[20:23], s38 offen offset:3072 lds\n"
    "v_mfma_f32_16x16x32_bf16 v[228:231], a[228:231], v[212:215], v[228:231]\n"
    "ds_read_b128 v[248:251], v43 offset:2048\n"
    "v_mfma_f32_16x16x32_bf16 v[160:163], a[40:43], a[48:51], v[160:163]\n"
    "ds_read_b64_tr_b16 a[20:21], v49 offset:49152\n"
    "v_mfma_f32_16x16x32_bf16 v[232:235], a[232:235], v[192:195], 0\n"
    "ds_read_b128 v[252:255], v43 offset:3072\n"
    "v_mfma_f32_16x16x32_bf16 v[164:167], a[40:43], a[52:55], v[164:167]\n"
    "ds_read_b64_tr_b16 a[22:23], v49 offset:49664\n"
    "v_mfma_f32_16x16x32_bf16 v[232:235], a[236:239], v[208:211], v[232:235]\n"
    "v_mfma_f32_16x16x32_bf16 v[136:139], a[32:35], a[56:59], v[136:139]\n"
    "ds_read_b128 a[8:11], v44 offset:36864\n"
    "v_mfma_f32_16x16x32_bf16 v[236:239], a[232:235], v[196:199], 0\n"
    "v_mfma_f32_16x16x32_bf16 v[152:155], a[36:39], a[56:59], v[152:155]\n"
    "ds_read_b64_tr_b16 a[24:25], v50 offset:49152\n"
    "s_add_u32 m0, s36, 16384\n"
    "s_bfe_u32 s38, s58, 0x40004\n"
    "s_lshl_b32 s38, s38, 18\n"
    "s_or_b32 s38, s38, 131072\n"
    "buffer_load_dwordx4 v16, s[24:27], s38 offen lds\n"
    "v_mfma_f32_16x16x32_bf16 v[236:239], a[236:239], v[212:215], v[236:239]\n"
    "v_mfma_f32_16x16x32_bf16 v[168:171], a[40:43], a[56:59], v[168:171]\n"
    "ds_read_b64_tr_b16 a[26:27], v50 offset:49664\n"
    "v_mfma_f32_16x16x32_bf16 v[176:179], a[44:47], a[48:51], v[176:179]\n"
    "ds_read_b128 a[12:15], v44 offset:38912\n"
    "v_mfma_f32_16x16x32_bf16 v[180:183], a[44:47], a[52:55], v[180:183]\n"
    "ds_read_b64_tr_b16 a[28:29], v51 offset:49152\n"
    "s_waitcnt lgkmcnt(12)\n"
    "v_add_f32 v224, v224, v240\n"
    "v_add_f32 v225, v225, v241\n"
    "v_mfma_f32_16x16x32_bf16 v[184:187], a[44:47], a[56:59], v[184:187]\n"
    "ds_read_b64_tr_b16 a[30:31], v51 offset:49664\n"
    "buffer_load_dwordx4 v17, s[24:27], s38 offen offset:1024 lds\n"
    "v_add_f32 v226, v226, v242\n"
    "v_add_f32 v227, v227, v243\n"
    "v_mfma_f32_16x16x32_bf16 v[140:143], a[32:35], a[60:63], v[140:143]\n"
    "s_waitcnt lgkmcnt(11)\n"
    "v_add_f32 v228, v228, v244\n"
    "v_add_f32 v229, v229, v245\n"
    "v_mfma_f32_16x16x32_bf16 v[156:159], a[36:39], a[60:63], v[156:159]\n"
    "v_add_f32 v230, v230, v246\n"
    "v_add_f32 v231, v231, v247\n"
    "v_mfma_f32_16x16x32_bf16 v[172:175], a[40:43], a[60:63], v[172:175]\n"
    "s_waitcnt lgkmcnt(9)\n"
    "v_add_f32 v232, v232, v248\n"
    "v_add_f32 v233, v233, v249\n"
    "v_mfma_f32_16x16x32_bf16 v[188:191], a[44:47], a[60:63], v[188:191]\n"
    "buffer_load_dwordx4 v18, s[24:27], s38 offen offset:2048 lds\n"
    "v_add_f32 v234, v234, v250\n"
    "v_add_f32 v235, v235, v251\n"
    "; ================ period 17 ================\n"
    "s_waitcnt lgkmcnt(3)\n"
    "v_mfma_f32_16x16x32_bf16 v[128:131], a[0:3], a[16:19], v[128:131]\n"
    "ds_read_b128 a[32:35], v45 offset:32768\n"
    "v_add_f32 v236, v236, v252\n"
    "v_mfma_f32_16x16x32_bf16 v[144:147], a[4:7], a[16:19], v[144:147]\n"
    "ds_read_b64_tr_b16 a[48:49], v48 offset:53248\n"
    "v_add_f32 v237, v237, v253\n"
    "v_add_f32 v238, v238, v254\n"
    "v_mfma_f32_16x16x32_bf16 v[132:135], a[0:3], a[20:23], v[132:135]\n"
    "ds_read_b64_tr_b16 a[50:51], v48 offset:53760\n"
    "v_add_f32 v239, v239, v255\n"
    "v_max3_f32 v56, v224, v225, v226\n"
    "v_mfma_f32_16x16x32_bf16 v[148:151], a[4:7], a[20:23], v[148:151]\n"
    "ds_read_b128 a[36:39], v45 offset:34816\n"
    "buffer_load_dwordx4 v19, s[24:27], s38 offen offset:3072 lds\n"
    "v_max3_f32 v57, v228, v229, v230\n"
    "v_max3_f32 v56, v56, v227, v232\n"
    "v_mfma_f32_16x16x32_bf16 v[160:163], a[8:11], a[16:19], v[160:163]\n"
    "ds_read_b64_tr_b16 a[52:53], v49 offset:53248\n"
    "v_max3_f32 v57, v57, v231, v236\n"
    "v_mfma_f32_16x16x32_bf16 v[164:167], a[8:11], a[20:23], v[164:167]\n"
    "ds_read_b64_tr_b16 a[54:55], v49 offset:53760\n"
    "v_max3_f32 v56, v56, v233, v234\n"
    "v_max3_f32 v57, v57, v237, v238\n"
    "v_mfma_f32_16x16x32_bf16 v[136:139], a[0:3], a[24:27], v[136:139]\n"
    "ds_read_b128 a[40:43], v45 offset:36864\n"
    "v_max_f32 v56, v56, v235\n"
    "v_max_f32 v57, v57, v239\n"
    "v_mfma_f32_16x16x32_bf16 v[152:155], a[4:7], a[24:27], v[152:155]\n"
    "ds_read_b64_tr_b16 a[56:57], v50 offset:53248\n"
    "s_add_u32 m0, s36, 49152\n"
    "s_bfe_u32 s38, s58, 0x40008\n"
    "s_lshl_b32 s38, s38, 13\n"
    "buffer_load_dwordx4 v12, s[20:23], s38 offen lds\n"
    "v_mov_b32 v58, v56\n"
    "v_mov_b32 v59, v57\n"
    "v_mfma_f32_16x16x32_bf16 v[168:171], a[8:11], a[24:27], v[168:171]\n"
    "ds_read_b64_tr_b16 a[58:59], v50 offset:53760\n"
    "v_permlane16_swap_b32 v56, v58\n"
    "s_waitcnt lgkmcnt(11)\n"
    "v_mfma_f32_16x16x32_bf16 v[176:179], a[12:15], a[16:19], v[176:179]\n"
    "ds_read_b128 a[44:47], v45 offset:38912\n"
    "v_permlane16_swap_b32 v57, v59\n"
    "v_max_f32 v56, v56, v58\n"
    "v_mfma_f32_16x16x32_bf16 v[180:183], a[12:15], a[20:23], v[180:183]\n"
    "ds_read_b64_tr_b16 a[60:61], v51 offset:53248\n"
    "v_max_f32 v57, v57, v59\n"
    "v_mov_b32 v58, v56\n"
    "v_mfma_f32_16x16x32_bf16 v[184:187], a[12:15], a[24:27], v[184:187]\n"
    "ds_read_b64_tr_b16 a[62:63], v51 offset:53760\n"
    "buffer_load_dwordx4 v13, s[20:23], s38 offen offset:1024 lds\n"
    "v_mov_b32 v59, v57\n"
    "v_permlane32_swap_b32 v56, v58\n"
    "s_waitcnt lgkmcnt(12)\n"
    "v_mfma_f32_16x16x32_bf16 v[140:143], a[0:3], a[28:31], v[140:143]\n"
    "v_permlane32_swap_b32 v57, v59\n"
    "v_mfma_f32_16x16x32_bf16 v[156:159], a[4:7], a[28:31], v[156:159]\n"
    "v_max_f32 v56, v56, v58\n"
    "v_max_f32 v57, v57, v59\n"
    "v_mfma_f32_16x16x32_bf16 v[172:175], a[8:11], a[28:31], v[172:175]\n"
    "v_mul_f32 v56, s39, v56\n"
    "v_mul_f32 v57, s39, v57\n"
    "v_mfma_f32_16x16x32_bf16 v[188:191], a[12:15], a[28:31], v[188:191]\n"
    "buffer_load_dwordx4 v14, s[20:23], s38 offen offset:2048 lds\n"
    "v_fma_f32 v224, v224, s39, -v56\n"
    "v_fma_f32 v228, v228, s39, -v57\n"
    "s_waitcnt vmcnt(11)\n"
    "s_barrier\n"
    "s_waitcnt lgkmcnt(3)\n"
    "v_mfma_f32_16x16x32_bf16 v[128:131], a[32:35], a[48:51], v[128:131]\n"
    "ds_read_b128 a[0:3], v46 offset:0\n"
    "v_fma_f32 v225, v225, s39, -v56\n"
    "v_mfma_f32_16x16x32_bf16 v[144:147], a[36:39], a[48:51], v[144:147]\n"
    "ds_read_b64_tr_b16 a[16:17], v52 offset:16384\n"
    "buffer_load_dwordx4 v15, s[20:23], s38 offen offset:3072 lds\n"
    "v_fma_f32 v229, v229, s39, -v57\n"
    "v_fma_f32 v226, v226, s39, -v56\n"
    "v_mfma_f32_16x16x32_bf16 v[132:135], a[32:35], a[52:55], v[132:135]\n"
    "ds_read_b64_tr_b16 a[18:19], v52 offset:16896\n"
    "v_fma_f32 v230, v230, s39, -v57\n"
    "v_fma_f32 v227, v227, s39, -v56\n"
    "v_mfma_f32_16x16x32_bf16 v[148:151], a[36:39], a[52:55], v[148:151]\n"
    "ds_read_b128 a[4:7], v46 offset:2048\n"
    "s_add_u32 m0, s36, 65536\n"
    "s_bfe_u32 s38, s58, 0x40008\n"
    "s_lshl_b32 s38, s38, 18\n"
    "buffer_load_dwordx4 v16, s[24:27], s38 offen lds\n"
    "v_fma_f32 v231, v231, s39, -v57\n"
    "v_fma_f32 v232, v232, s39, -v56\n"
    "v_mfma_f32_16x16x32_bf16 v[160:163], a[40:43], a[48:51], v[160:163]\n"
    "ds_read_b64_tr_b16 a[20:21], v53 offset:16384\n"
    "v_fma_f32 v236, v236, s39, -v57\n"
    "v_mfma_f32_16x16x32_bf16 v[164:167], a[40:43], a[52:55], v[164:167]\n"
    "ds_read_b64_tr_b16 a[22:23], v53 offset:16896\n"
    "v_fma_f32 v233, v233, s39, -v56\n"
    "v_fma_f32 v237, v237, s39, -v57\n"
    "v_mfma_f32_16x16x32_bf16 v[136:139], a[32:35], a[56:59], v[136:139]\n"
    "ds_read_b128 a[8:11], v46 offset:4096\n"
    "v_fma_f32 v234, v234, s39, -v56\n"
    "v_fma_f32 v238, v238, s39, -v57\n"
    "v_mfma_f32_16x16x32_bf16 v[152:155], a[36:39], a[56:59], v[152:155]\n"
    "ds_read_b64_tr_b16 a[24:25], v54 offset:16384\n"
    "buffer_load_dwordx4 v17, s[24:27], s38 offen offset:1024 lds\n"
    "v_fma_f32 v235, v235, s39, -v56\n"
    "v_fma_f32 v239, v239, s39, -v57\n"
    "v_mfma_f32_16x16x32_bf16 v[168:171], a[40:43], a[56:59], v[168:171]\n"
    "ds_read_b64_tr_b16 a[26:27], v54 offset:16896\n"
    "v_exp_f32 v224, v224\n"
    "s_waitcnt lgkmcnt(11)\n"
    "v_mfma_f32_16x16x32_bf16 v[176:179], a[44:47], a[48:51], v[176:179]\n"
    "ds_read_b128 a[12:15], v46 offset:6144\n"
    "v_exp_f32 v228, v228\n"
    "v_exp_f32 v225, v225\n"
    "v_mfma_f32_16x16x32_bf16 v[180:183], a[44:47], a[52:55], v[180:183]\n"
    "ds_read_b64_tr_b16 a[28:29], v55 offset:16384\n"
    "v_exp_f32 v229, v229\n"
    "v_exp_f32 v226, v226\n"
    "v_mfma_f32_16x16x32_bf16 v[184:187], a[44:47], a[56:59], v[184:187]\n"
    "ds_read_b64_tr_b16 a[30:31], v55 offset:16896\n"
    "buffer_load_dwordx4 v18, s[24:27], s38 offen offset:2048 lds\n"
    "v_exp_f32 v230, v230\n"
    "v_add_f32 v60, v224, v225\n"
    "s_waitcnt lgkmcnt(12)\n"
    "v_mfma_f32_16x16x32_bf16 v[140:143], a[32:35], a[60:63], v[140:143]\n"
    "v_add_f32 v61, v228, v229\n"
    "v_mfma_f32_16x16x32_bf16 v[156:159], a[36:39], a[60:63], v[156:159]\n"
    "v_exp_f32 v227, v227\n"
    "v_exp_f32 v231, v231\n"
    "v_mfma_f32_16x16x32_bf16 v[172:175], a[40:43], a[60:63], v[172:175]\n"
    "v_add_f32 v60, v60, v226\n"
    "v_add_f32 v61, v61, v230\n"
    "v_mfma_f32_16x16x32_bf16 v[188:191], a[44:47], a[60:63], v[188:191]\n"
    "buffer_load_dwordx4 v19, s[24:27], s38 offen offset:3072 lds\n"
    "v_exp_f32 v232, v232\n"
    "v_exp_f32 v236, v236\n"
    "; ================ period 18 ================\n"
    "s_waitcnt lgkmcnt(3)\n"
    "v_mfma_f32_16x16x32_bf16 v[64:67], a[0:3], a[16:19], 0\n"
    "ds_read_b128 a[32:35], v47 offset:0\n"
    "v_add_f32 v60, v60, v227\n"
    "v_mfma_f32_16x16x32_bf16 v[80:83], a[4:7], a[16:19], 0\n"
    "ds_read_b64_tr_b16 a[48:49], v52 offset:20480\n"
    "v_add_f32 v61, v61, v231\n"
    "v_exp_f32 v233, v233\n"
    "v_mfma_f32_16x16x32_bf16 v[68:71], a[0:3], a[20:23], 0\n"
    "ds_read_b64_tr_b16 a[50:51], v52 offset:20992\n"
    "v_exp_f32 v237, v237\n"
    "v_add_f32 v60, v60, v232\n"
    "v_mfma_f32_16x16x32_bf16 v[84:87], a[4:7], a[20:23], 0\n"
    "ds_read_b128 a[36:39], v47 offset:2048\n"
    "s_add_u32 m0, s36, 81920\n"
    "s_bfe_u32 s38, s58, 0x40008\n"
    "s_lshl_b32 s38, s38, 13\n"
    "s_or_b32 s38, s38, 128\n"
    "buffer_load_dwordx4 v12, s[20:23], s38 offen lds\n"
    "v_add_f32 v61, v61, v236\n"
    "v_exp_f32 v234, v234\n"
    "v_mfma_f32_16x16x32_bf16 v[96:99], a[8:11], a[16:19], 0\n"
    "ds_read_b64_tr_b16 a[52:53], v53 offset:20480\n"
    "v_exp_f32 v238, v238\n"
    "v_mfma_f32_16x16x32_bf16 v[100:103], a[8:11], a[20:23], 0\n"
    "ds_read_b64_tr_b16 a[54:55], v53 offset:20992\n"
    "v_add_f32 v60, v60, v233\n"
    "v_add_f32 v61, v61, v237\n"
    "v_mfma_f32_16x16x32_bf16 v[72:75], a[0:3], a[24:27], 0\n"
    "ds_read_b128 a[40:43], v47 offset:4096\n"
    "v_exp_f32 v235, v235\n"
    "v_exp_f32 v239, v239\n"
    "v_mfma_f32_16x16x32_bf16 v[88:91], a[4:7], a[24:27], 0\n"
    "ds_read_b64_tr_b16 a[56:57], v54 offset:20480\n"
    "buffer_load_dwordx4 v13, s[20:23], s38 offen offset:1024 lds\n"
    "v_add_f32 v60, v60, v234\n"
    "v_add_f32 v61, v61, v238\n"
    "v_mfma_f32_16x16x32_bf16 v[104:107], a[8:11], a[24:27], 0\n"
    "ds_read_b64_tr_b16 a[58:59], v54 offset:20992\n"
    "v_add_f32 v60, v60, v235\n"
    "s_waitcnt lgkmcnt(11)\n"
    "v_mfma_f32_16x16x32_bf16 v[112:115], a[12:15], a[16:19], 0\n"
    "ds_read_b128 a[44:47], v47 offset:6144\n"
    "v_add_f32 v61, v61, v239\n"
    "v_mov_b32 v58, v60\n"
    "v_mfma_f32_16x16x32_bf16 v[116:119], a[12:15], a[20:23], 0\n"
    "ds_read_b64_tr_b16 a[60:61], v55 offset:20480\n"
    "v_mov_b32 v59, v61\n"
    "v_permlane16_swap_b32 v60, v58\n"
    "v_mfma_f32_16x16x32_bf16 v[120:123], a[12:15], a[24:27], 0\n"
    "ds_read_b64_tr_b16 a[62:63], v55 offset:20992\n"
    "buffer_load_dwordx4 v14, s[20:23], s38 offen offset:2048 lds\n"
    "v_permlane16_swap_b32 v61, v59\n"
    "v_add_f32 v60, v60, v58\n"
    "s_waitcnt lgkmcnt(12)\n"
    "v_mfma_f32_16x16x32_bf16 v[76:79], a[0:3], a[28:31], 0\n"
    "v_add_f32 v61, v61, v59\n"
    "v_mfma_f32_16x16x32_bf16 v[92:95], a[4:7], a[28:31], 0\n"
    "v_mov_b32 v58, v60\n"
    "v_mov_b32 v59, v61\n"
    "v_mfma_f32_16x16x32_bf16 v[108:111], a[8:11], a[28:31], 0\n"
    "v_permlane32_swap_b32 v60, v58\n"
    "v_permlane32_swap_b32 v61, v59\n"
    "v_mfma_f32_16x16x32_bf16 v[124:127], a[12:15], a[28:31], 0\n"
    "buffer_load_dwordx4 v15, s[20:23], s38 offen offset:3072 lds\n"
    "v_add_f32 v60, v60, v58\n"
    "v_add_f32 v61, v61, v59\n"
    "s_waitcnt vmcnt(12)\n"
    "s_barrier\n"
    "s_waitcnt lgkmcnt(3)\n"
    "v_mfma_f32_16x16x32_bf16 v[64:67], a[32:35], a[48:51], v[64:67]\n"
    "ds_read_b128 a[0:3], v4 offset:0\n"
    "v_rcp_f32 v60, v60\n"
    "v_mfma_f32_16x16x32_bf16 v[80:83], a[36:39], a[48:51], v[80:83]\n"
    "ds_read_b64_tr_b16 a[16:17], v6 offset:16384\n"
    "v_rcp_f32 v61, v61\n"
    "v_mul_f32 v224, v224, v60\n"
    "v_mfma_f32_16x16x32_bf16 v[68:71], a[32:35], a[52:55], v[68:71]\n"
    "ds_read_b64_tr_b16 a[18:19], v6 offset:16896\n"
    "v_mul_f32 v228, v228, v61\n"
    "v_mul_f32 v225, v225, v60\n"
    "v_mfma_f32_16x16x32_bf16 v[84:87], a[36:39], a[52:55], v[84:87]\n"
    "ds_read_b128 a[4:7], v4 offset:2048\n"
    "s_add_u32 m0, s36, 98304\n"
    "s_bfe_u32 s38, s58, 0x40008\n"
    "s_lshl_b32 s38, s38, 18\n"
    "s_or_b32 s38, s38, 131072\n"
    "buffer_load_dwordx4 v16, s[24:27], s38 offen lds\n"
    "v_mul_f32 v229, v229, v61\n"
    "v_mul_f32 v226, v226, v60\n"
    "v_mfma_f32_16x16x32_bf16 v[96:99], a[40:43], a[48:51], v[96:99]\n"
    "ds_read_b64_tr_b16 a[20:21], v7 offset:16384\n"
    "v_mul_f32 v230, v230, v61\n"
    "v_mfma_f32_16x16x32_bf16 v[100:103], a[40:43], a[52:55], v[100:103]\n"
    "ds_read_b64_tr_b16 a[22:23], v7 offset:16896\n"
    "v_mul_f32 v227, v227, v60\n"
    "v_mul_f32 v231, v231, v61\n"
    "v_mfma_f32_16x16x32_bf16 v[72:75], a[32:35], a[56:59], v[72:75]\n"
    "ds_read_b128 a[8:11], v4 offset:4096\n"
    "v_mul_f32 v232, v232, v60\n"
    "v_mul_f32 v236, v236, v61\n"
    "v_mfma_f32_16x16x32_bf16 v[88:91], a[36:39], a[56:59], v[88:91]\n"
    "ds_read_b64_tr_b16 a[24:25], v8 offset:16384\n"
    "buffer_load_dwordx4 v17, s[24:27], s38 offen offset:1024 lds\n"
    "v_mul_f32 v233, v233, v60\n"
    "v_mul_f32 v237, v237, v61\n"
    "v_mfma_f32_16x16x32_bf16 v[104:107], a[40:43], a[56:59], v[104:107]\n"
    "ds_read_b64_tr_b16 a[26:27], v8 offset:16896\n"
    "v_mul_f32 v234, v234, v60\n"
    "s_waitcnt lgkmcnt(11)\n"
    "v_mfma_f32_16x16x32_bf16 v[112:115], a[44:47], a[48:51], v[112:115]\n"
    "ds_read_b128 a[12:15], v4 offset:6144\n"
    "v_mul_f32 v238, v238, v61\n"
    "v_mul_f32 v235, v235, v60\n"
    "v_mfma_f32_16x16x32_bf16 v[116:119], a[44:47], a[52:55], v[116:119]\n"
    "ds_read_b64_tr_b16 a[28:29], v9 offset:16384\n"
    "v_mul_f32 v239, v239, v61\n"
    "v_cvt_pk_bf16_f32 v240, v224, v225\n"
    "v_mfma_f32_16x16x32_bf16 v[120:123], a[44:47], a[56:59], v[120:123]\n"
    "ds_read_b64_tr_b16 a[30:31], v9 offset:16896\n"
    "buffer_load_dwordx4 v18, s[24:27], s38 offen offset:2048 lds\n"
    "v_cvt_pk_bf16_f32 v241, v226, v227\n"
    "v_cvt_pk_bf16_f32 v242, v232, v233\n"
    "s_waitcnt lgkmcnt(12)\n"
    "v_mfma_f32_16x16x32_bf16 v[76:79], a[32:35], a[60:63], v[76:79]\n"
    "v_cvt_pk_bf16_f32 v243, v234, v235\n"
    "v_mfma_f32_16x16x32_bf16 v[92:95], a[36:39], a[60:63], v[92:95]\n"
    "v_cvt_pk_bf16_f32 v244, v228, v229\n"
    "v_cvt_pk_bf16_f32 v245, v230, v231\n"
    "v_mfma_f32_16x16x32_bf16 v[108:111], a[40:43], a[60:63], v[108:111]\n"
    "v_cvt_pk_bf16_f32 v246, v236, v237\n"
    "v_cvt_pk_bf16_f32 v247, v238, v239\n"
    "v_mfma_f32_16x16x32_bf16 v[124:127], a[44:47], a[60:63], v[124:127]\n"
    "buffer_load_dwordx4 v19, s[24:27], s38 offen offset:3072 lds\n"
    "buffer_store_dwordx4 v[240:243], v21, s[28:31], 0 offen sc1\n"
    "buffer_store_dwordx4 v[244:247], v21, s[28:31], 0 offen offset:1024 sc1\n"
    "; ================ period 19 ================\n"
    "s_waitcnt lgkmcnt(3)\n"
    "v_mfma_f32_16x16x32_bf16 v[64:67], a[0:3], a[16:19], v[64:67]\n"
    "ds_read_b128 a[32:35], v5 offset:0\n"
    "v_cvt_pk_bf16_f32 v192, v128, v129\n"
    "v_mfma_f32_16x16x32_bf16 v[80:83], a[4:7], a[16:19], v[80:83]\n"
    "ds_read_b64_tr_b16 a[48:49], v6 offset:20480\n"
    "v_cvt_pk_bf16_f32 v193, v130, v131\n"
    "v_cvt_pk_bf16_f32 v194, v144, v145\n"
    "v_mfma_f32_16x16x32_bf16 v[68:71], a[0:3], a[20:23], v[68:71]\n"
    "ds_read_b64_tr_b16 a[50:51], v6 offset:20992\n"
    "v_cvt_pk_bf16_f32 v195, v146, v147\n"
    "v_cvt_pk_bf16_f32 v196, v132, v133\n"
    "v_mfma_f32_16x16x32_bf16 v[84:87], a[4:7], a[20:23], v[84:87]\n"
    "ds_read_b128 a[36:39], v5 offset:2048\n"
    "s_add_u32 m0, s36, 114688\n"
    "s_bfe_u32 s38, s58, 0x4000c\n"
    "s_lshl_b32 s38, s38, 13\n"
    "buffer_load_dwordx4 v12, s[20:23], s38 offen lds\n"
    "v_cvt_pk_bf16_f32 v197, v134, v135\n"
    "v_cvt_pk_bf16_f32 v198, v148, v149\n"
    "v_mfma_f32_16x16x32_bf16 v[96:99], a[8:11], a[16:19], v[96:99]\n"
    "ds_read_b64_tr_b16 a[52:53], v7 offset:20480\n"
    "v_cvt_pk_bf16_f32 v199, v150, v151\n"
    "v_mfma_f32_16x16x32_bf16 v[100:103], a[8:11], a[20:23], v[100:103]\n"
    "ds_read_b64_tr_b16 a[54:55], v7 offset:20992\n"
    "v_cvt_pk_bf16_f32 v200, v136, v137\n"
    "v_cvt_pk_bf16_f32 v201, v138, v139\n"
    "v_mfma_f32_16x16x32_bf16 v[72:75], a[0:3], a[24:27], v[72:75]\n"
    "ds_read_b128 a[40:43], v5 offset:4096\n"
    "v_cvt_pk_bf16_f32 v202, v152, v153\n"
    "v_cvt_pk_bf16_f32 v203, v154, v155\n"
    "v_mfma_f32_16x16x32_bf16 v[88:91], a[4:7], a[24:27], v[88:91]\n"
    "ds_read_b64_tr_b16 a[56:57], v8 offset:20480\n"
    "buffer_load_dwordx4 v13, s[20:23], s38 offen offset:1024 lds\n"
    "v_cvt_pk_bf16_f32 v208, v160, v161\n"
    "v_cvt_pk_bf16_f32 v209, v162, v163\n"
    "v_mfma_f32_16x16x32_bf16 v[104:107], a[8:11], a[24:27], v[104:107]\n"
    "ds_read_b64_tr_b16 a[58:59], v8 offset:20992\n"
    "v_cvt_pk_bf16_f32 v210, v176, v177\n"
    "s_waitcnt lgkmcnt(11)\n"
    "v_mfma_f32_16x16x32_bf16 v[112:115], a[12:15], a[16:19], v[112:115]\n"
    "ds_read_b128 a[44:47], v5 offset:6144\n"
    "v_cvt_pk_bf16_f32 v211, v178, v179\n"
    "v_cvt_pk_bf16_f32 v212, v164, v165\n"
    "v_mfma_f32_16x16x32_bf16 v[116:119], a[12:15], a[20:23], v[116:119]\n"
    "ds_read_b64_tr_b16 a[60:61], v9 offset:20480\n"
    "v_cvt_pk_bf16_f32 v213, v166, v167\n"
    "v_cvt_pk_bf16_f32 v214, v180, v181\n"
    "v_mfma_f32_16x16x32_bf16 v[120:123], a[12:15], a[24:27], v[120:123]\n"
    "ds_read_b64_tr_b16 a[62:63], v9 offset:20992\n"
    "buffer_load_dwordx4 v14, s[20:23], s38 offen offset:2048 lds\n"
    "v_cvt_pk_bf16_f32 v215, v182, v183\n"
    "v_cvt_pk_bf16_f32 v216, v168, v169\n"
    "s_waitcnt lgkmcnt(12)\n"
    "v_mfma_f32_16x16x32_bf16 v[76:79], a[0:3], a[28:31], v[76:79]\n"
    "v_cvt_pk_bf16_f32 v217, v170, v171\n"
    "v_mfma_f32_16x16x32_bf16 v[92:95], a[4:7], a[28:31], v[92:95]\n"
    "v_cvt_pk_bf16_f32 v218, v184, v185\n"
    "v_cvt_pk_bf16_f32 v219, v186, v187\n"
    "v_mfma_f32_16x16x32_bf16 v[108:111], a[8:11], a[28:31], v[108:111]\n"
    "v_cvt_pk_bf16_f32 v204, v140, v141\n"
    "v_cvt_pk_bf16_f32 v205, v142, v143\n"
    "v_mfma_f32_16x16x32_bf16 v[124:127], a[12:15], a[28:31], v[124:127]\n"
    "buffer_load_dwordx4 v15, s[20:23], s38 offen offset:3072 lds\n"
    "v_cvt_pk_bf16_f32 v206, v156, v157\n"
    "v_cvt_pk_bf16_f32 v207, v158, v159\n"
    "s_waitcnt vmcnt(14)\n"
    "s_barrier\n"
    "s_waitcnt lgkmcnt(3)\n"
    "v_mfma_f32_16x16x32_bf16 v[64:67], a[32:35], a[48:51], v[64:67]\n"
    "ds_read_b128 a[0:3], v4 offset:49152\n"
    "v_cvt_pk_bf16_f32 v220, v172, v173\n"
    "v_mfma_f32_16x16x32_bf16 v[80:83], a[36:39], a[48:51], v[80:83]\n"
    "ds_read_b64_tr_b16 a[16:17], v48 offset:0\n"
    "v_cvt_pk_bf16_f32 v221, v174, v175\n"
    "v_cvt_pk_bf16_f32 v222, v188, v189\n"
    "v_mfma_f32_16x16x32_bf16 v[68:71], a[32:35], a[52:55], v[68:71]\n"
    "ds_read_b64_tr_b16 a[18:19], v48 offset:512\n"
    "v_cvt_pk_bf16_f32 v223, v190, v191\n"
    "v_mfma_f32_16x16x32_bf16 v[84:87], a[36:39], a[52:55], v[84:87]\n"
    "ds_read_b128 a[4:7], v4 offset:51200\n"
    "s_add_u32 m0, s36, 131072\n"
    "s_bfe_u32 s38, s58, 0x4000c\n"
    "s_lshl_b32 s38, s38, 18\n"
    "buffer_load_dwordx4 v16, s[24:27], s38 offen lds\n"
    "v_mfma_f32_16x16x32_bf16 v[96:99], a[40:43], a[48:51], v[96:99]\n"
    "ds_read_b64_tr_b16 a[20:21], v49 offset:0\n"
    "v_mfma_f32_16x16x32_bf16 v[100:103], a[40:43], a[52:55], v[100:103]\n"
    "ds_read_b64_tr_b16 a[22:23], v49 offset:512\n"
    "v_mfma_f32_16x16x32_bf16 v[72:75], a[32:35], a[56:59], v[72:75]\n"
    "ds_read_b128 a[8:11], v4 offset:53248\n"
    "v_mfma_f32_16x16x32_bf16 v[88:91], a[36:39], a[56:59], v[88:91]\n"
    "ds_read_b64_tr_b16 a[24:25], v50 offset:0\n"
    "buffer_load_dwordx4 v17, s[24:27], s38 offen offset:1024 lds\n"
    "v_mfma_f32_16x16x32_bf16 v[104:107], a[40:43], a[56:59], v[104:107]\n"
    "ds_read_b64_tr_b16 a[26:27], v50 offset:512\n"
    "s_waitcnt lgkmcnt(11)\n"
    "v_mfma_f32_16x16x32_bf16 v[112:115], a[44:47], a[48:51], v[112:115]\n"
    "ds_read_b128 a[12:15], v4 offset:55296\n"
    "v_mfma_f32_16x16x32_bf16 v[116:119], a[44:47], a[52:55], v[116:119]\n"
    "ds_read_b64_tr_b16 a[28:29], v51 offset:0\n"
    "v_mfma_f32_16x16x32_bf16 v[120:123], a[44:47], a[56:59], v[120:123]\n"
    "ds_read_b64_tr_b16 a[30:31], v51 offset:512\n"
    "buffer_load_dwordx4 v18, s[24:27], s38 offen offset:2048 lds\n"
    "s_waitcnt lgkmcnt(12)\n"
    "v_mfma_f32_16x16x32_bf16 v[76:79], a[32:35], a[60:63], v[76:79]\n"
    "v_mfma_f32_16x16x32_bf16 v[92:95], a[36:39], a[60:63], v[92:95]\n"
    "v_mfma_f32_16x16x32_bf16 v[108:111], a[40:43], a[60:63], v[108:111]\n"
    "v_mfma_f32_16x16x32_bf16 v[124:127], a[44:47], a[60:63], v[124:127]\n"
    "buffer_load_dwordx4 v19, s[24:27], s38 offen offset:3072 lds\n"
    "; ================ period 20 ================\n"
    "s_waitcnt lgkmcnt(3)\n"
    "v_mfma_f32_16x16x32_bf16 v[128:131], a[0:3], a[16:19], 0\n"
    "ds_read_b128 a[32:35], v5 offset:49152\n"
    "v_cvt_pk_bf16_f32 v224, v64, v65\n"
    "v_mfma_f32_16x16x32_bf16 v[144:147], a[4:7], a[16:19], 0\n"
    "ds_read_b64_tr_b16 a[48:49], v48 offset:4096\n"
    "v_cvt_pk_bf16_f32 v225, v66, v67\n"
    "v_mfma_f32_16x16x32_bf16 v[132:135], a[0:3], a[20:23], 0\n"
    "ds_read_b64_tr_b16 a[50:51], v48 offset:4608\n"
    "v_cvt_pk_bf16_f32 v226, v80, v81\n"
    "v_mfma_f32_16x16x32_bf16 v[148:151], a[4:7], a[20:23], 0\n"
    "ds_read_b128 a[36:39], v5 offset:51200\n"
    "s_add_u32 m0, s36, 147456\n"
    "s_bfe_u32 s38, s58, 0x4000c\n"
    "s_lshl_b32 s38, s38, 13\n"
    "s_or_b32 s38, s38, 128\n"
    "buffer_load_dwordx4 v12, s[20:23], s38 offen lds\n"
    "v_cvt_pk_bf16_f32 v227, v82, v83\n"
    "v_mfma_f32_16x16x32_bf16 v[160:163], a[8:11], a[16:19], 0\n"
    "ds_read_b64_tr_b16 a[52:53], v49 offset:4096\n"
    "v_cvt_pk_bf16_f32 v228, v68, v69\n"
    "v_mfma_f32_16x16x32_bf16 v[164:167], a[8:11], a[20:23], 0\n"
    "ds_read_b64_tr_b16 a[54:55], v49 offset:4608\n"
    "v_cvt_pk_bf16_f32 v229, v70, v71\n"
    "v_mfma_f32_16x16x32_bf16 v[136:139], a[0:3], a[24:27], 0\n"
    "ds_read_b128 a[40:43], v5 offset:53248\n"
    "v_cvt_pk_bf16_f32 v230, v84, v85\n"
    "v_mfma_f32_16x16x32_bf16 v[152:155], a[4:7], a[24:27], 0\n"
    "ds_read_b64_tr_b16 a[56:57], v50 offset:4096\n"
    "buffer_load_dwordx4 v13, s[20:23], s38 offen offset:1024 lds\n"
    "v_cvt_pk_bf16_f32 v231, v86, v87\n"
    "v_mfma_f32_16x16x32_bf16 v[168:171], a[8:11], a[24:27], 0\n"
    "ds_read_b64_tr_b16 a[58:59], v50 offset:4608\n"
    "v_cvt_pk_bf16_f32 v232, v72, v73\n"
    "s_waitcnt lgkmcnt(11)\n"
    "v_mfma_f32_16x16x32_bf16 v[176:179], a[12:15], a[16:19], 0\n"
    "ds_read_b128 a[44:47], v5 offset:55296\n"
    "v_cvt_pk_bf16_f32 v233, v74, v75\n"
    "v_mfma_f32_16x16x32_bf16 v[180:183], a[12:15], a[20:23], 0\n"
    "ds_read_b64_tr_b16 a[60:61], v51 offset:4096\n"
    "v_cvt_pk_bf16_f32 v234, v88, v89\n"
    "v_mfma_f32_16x16x32_bf16 v[184:187], a[12:15], a[24:27], 0\n"
    "ds_read_b64_tr_b16 a[62:63], v51 offset:4608\n"
    "buffer_load_dwordx4 v14, s[20:23], s38 offen offset:2048 lds\n"
    "v_cvt_pk_bf16_f32 v235, v90, v91\n"
    "s_waitcnt lgkmcnt(12)\n"
    "v_mfma_f32_16x16x32_bf16 v[140:143], a[0:3], a[28:31], 0\n"
    "v_cvt_pk_bf16_f32 v240, v96, v97\n"
    "v_mfma_f32_16x16x32_bf16 v[156:159], a[4:7], a[28:31], 0\n"
    "v_cvt_pk_bf16_f32 v241, v98, v99\n"
    "v_mfma_f32_16x16x32_bf16 v[172:175], a[8:11], a[28:31], 0\n"
    "v_cvt_pk_bf16_f32 v242, v112, v113\n"
    "v_mfma_f32_16x16x32_bf16 v[188:191], a[12:15], a[28:31], 0\n"
    "buffer_load_dwordx4 v15, s[20:23], s38 offen offset:3072 lds\n"
    "v_cvt_pk_bf16_f32 v243, v114, v115\n"
    "s_waitcnt vmcnt(14)\n"
    "s_barrier\n"
    "s_waitcnt lgkmcnt(3)\n"
    "v_mfma_f32_16x16x32_bf16 v[128:131], a[32:35], a[48:51], v[128:131]\n"
    "ds_read_b128 a[0:3], v44 offset:16384\n"
    "v_cvt_pk_bf16_f32 v244, v100, v101\n"
    "v_mfma_f32_16x16x32_bf16 v[144:147], a[36:39], a[48:51], v[144:147]\n"
    "ds_read_b64_tr_b16 a[16:17], v48 offset:32768\n"
    "s_add_u32 m0, s36, 0\n"
    "s_bfe_u32 s38, s58, 0x4000c\n"
    "s_lshl_b32 s38, s38, 18\n"
    "s_or_b32 s38, s38, 131072\n"
    "buffer_load_dwordx4 v16, s[24:27], s38 offen lds\n"
    "v_cvt_pk_bf16_f32 v245, v102, v103\n"
    "v_mfma_f32_16x16x32_bf16 v[132:135], a[32:35], a[52:55], v[132:135]\n"
    "ds_read_b64_tr_b16 a[18:19], v48 offset:33280\n"
    "v_cvt_pk_bf16_f32 v246, v116, v117\n"
    "v_mfma_f32_16x16x32_bf16 v[148:151], a[36:39], a[52:55], v[148:151]\n"
    "ds_read_b128 a[4:7], v44 offset:18432\n"
    "buffer_load_dwordx4 v17, s[24:27], s38 offen offset:1024 lds\n"
    "buffer_load_dwordx4 v18, s[24:27], s38 offen offset:2048 lds\n"
    "v_cvt_pk_bf16_f32 v247, v118, v119\n"
    "v_mfma_f32_16x16x32_bf16 v[160:163], a[40:43], a[48:51], v[160:163]\n"
    "ds_read_b64_tr_b16 a[20:21], v49 offset:32768\n"
    "v_cvt_pk_bf16_f32 v248, v104, v105\n"
    "v_mfma_f32_16x16x32_bf16 v[164:167], a[40:43], a[52:55], v[164:167]\n"
    "ds_read_b64_tr_b16 a[22:23], v49 offset:33280\n"
    "buffer_load_dwordx4 v19, s[24:27], s38 offen offset:3072 lds\n"
    "v_cvt_pk_bf16_f32 v249, v106, v107\n"
    "v_mfma_f32_16x16x32_bf16 v[136:139], a[32:35], a[56:59], v[136:139]\n"
    "ds_read_b128 a[8:11], v44 offset:20480\n"
    "v_cvt_pk_bf16_f32 v250, v120, v121\n"
    "v_mfma_f32_16x16x32_bf16 v[152:155], a[36:39], a[56:59], v[152:155]\n"
    "ds_read_b64_tr_b16 a[24:25], v50 offset:32768\n"
    "s_add_u32 m0, s36, 16384\n"
    "s_bfe_u32 s38, s58, 0x40010\n"
    "s_lshl_b32 s38, s38, 13\n"
    "buffer_load_dwordx4 v12, s[20:23], s38 offen lds\n"
    "v_cvt_pk_bf16_f32 v251, v122, v123\n"
    "v_mfma_f32_16x16x32_bf16 v[168:171], a[40:43], a[56:59], v[168:171]\n"
    "ds_read_b64_tr_b16 a[26:27], v50 offset:33280\n"
    "v_cvt_pk_bf16_f32 v236, v76, v77\n"
    "s_waitcnt lgkmcnt(11)\n"
    "v_mfma_f32_16x16x32_bf16 v[176:179], a[44:47], a[48:51], v[176:179]\n"
    "ds_read_b128 a[12:15], v44 offset:22528\n"
    "v_cvt_pk_bf16_f32 v237, v78, v79\n"
    "v_mfma_f32_16x16x32_bf16 v[180:183], a[44:47], a[52:55], v[180:183]\n"
    "ds_read_b64_tr_b16 a[28:29], v51 offset:32768\n"
    "v_cvt_pk_bf16_f32 v238, v92, v93\n"
    "v_mfma_f32_16x16x32_bf16 v[184:187], a[44:47], a[56:59], v[184:187]\n"
    "ds_read_b64_tr_b16 a[30:31], v51 offset:33280\n"
    "buffer_load_dwordx4 v13, s[20:23], s38 offen offset:1024 lds\n"
    "v_cvt_pk_bf16_f32 v239, v94, v95\n"
    "s_waitcnt lgkmcnt(12)\n"
    "v_mfma_f32_16x16x32_bf16 v[140:143], a[32:35], a[60:63], v[140:143]\n"
    "v_cvt_pk_bf16_f32 v252, v108, v109\n"
    "v_mfma_f32_16x16x32_bf16 v[156:159], a[36:39], a[60:63], v[156:159]\n"
    "v_cvt_pk_bf16_f32 v253, v110, v111\n"
    "v_mfma_f32_16x16x32_bf16 v[172:175], a[40:43], a[60:63], v[172:175]\n"
    "v_cvt_pk_bf16_f32 v254, v124, v125\n"
    "v_mfma_f32_16x16x32_bf16 v[188:191], a[44:47], a[60:63], v[188:191]\n"
    "buffer_load_dwordx4 v14, s[20:23], s38 offen offset:2048 lds\n"
    "v_cvt_pk_bf16_f32 v255, v126, v127\n"
    "; ================ period 21 ================\n"
    "s_waitcnt lgkmcnt(3)\n"
    "v_mfma_f32_16x16x32_bf16 v[128:131], a[0:3], a[16:19], v[128:131]\n"
    "ds_read_b128 a[32:35], v45 offset:16384\n"
    "v_mfma_f32_16x16x32_bf16 v[144:147], a[4:7], a[16:19], v[144:147]\n"
    "ds_read_b64_tr_b16 a[48:49], v48 offset:36864\n"
    "v_mfma_f32_16x16x32_bf16 v[132:135], a[0:3], a[20:23], v[132:135]\n"
    "ds_read_b64_tr_b16 a[50:51], v48 offset:37376\n"
    "v_mfma_f32_16x16x32_bf16 v[148:151], a[4:7], a[20:23], v[148:151]\n"
    "ds_read_b128 a[36:39], v45 offset:18432\n"
    "buffer_load_dwordx4 v15, s[20:23], s38 offen offset:3072 lds\n"
    "v_mfma_f32_16x16x32_bf16 v[160:163], a[8:11], a[16:19], v[160:163]\n"
    "ds_read_b64_tr_b16 a[52:53], v49 offset:36864\n"
    "v_mfma_f32_16x16x32_bf16 v[164:167], a[8:11], a[20:23], v[164:167]\n"
    "ds_read_b64_tr_b16 a[54:55], v49 offset:37376\n"
    "v_mfma_f32_16x16x32_bf16 v[136:139], a[0:3], a[24:27], v[136:139]\n"
    "ds_read_b128 a[40:43], v45 offset:20480\n"
    "v_mfma_f32_16x16x32_bf16 v[152:155], a[4:7], a[24:27], v[152:155]\n"
    "ds_read_b64_tr_b16 a[56:57], v50 offset:36864\n"
    "s_add_u32 m0, s36, 32768\n"
    "s_bfe_u32 s38, s58, 0x40010\n"
    "s_lshl_b32 s38, s38, 18\n"
    "buffer_load_dwordx4 v16, s[24:27], s38 offen lds\n"
    "v_mfma_f32_16x16x32_bf16 v[168:171], a[8:11], a[24:27], v[168:171]\n"
    "ds_read_b64_tr_b16 a[58:59], v50 offset:37376\n"
    "s_waitcnt lgkmcnt(11)\n"
    "v_mfma_f32_16x16x32_bf16 v[176:179], a[12:15], a[16:19], v[176:179]\n"
    "ds_read_b128 a[44:47], v45 offset:22528\n"
    "v_mfma_f32_16x16x32_bf16 v[180:183], a[12:15], a[20:23], v[180:183]\n"
    "ds_read_b64_tr_b16 a[60:61], v51 offset:36864\n"
    "v_mfma_f32_16x16x32_bf16 v[184:187], a[12:15], a[24:27], v[184:187]\n"
    "ds_read_b64_tr_b16 a[62:63], v51 offset:37376\n"
    "buffer_load_dwordx4 v17, s[24:27], s38 offen offset:1024 lds\n"
    "s_waitcnt lgkmcnt(12)\n"
    "v_mfma_f32_16x16x32_bf16 v[140:143], a[0:3], a[28:31], v[140:143]\n"
    "v_mfma_f32_16x16x32_bf16 v[156:159], a[4:7], a[28:31], v[156:159]\n"
    "v_mfma_f32_16x16x32_bf16 v[172:175], a[8:11], a[28:31], v[172:175]\n"
    "v_mfma_f32_16x16x32_bf16 v[188:191], a[12:15], a[28:31], v[188:191]\n"
    "buffer_load_dwordx4 v18, s[24:27], s38 offen offset:2048 lds\n"
    "s_waitcnt vmcnt(15)\n"
    "s_mov_b64 exec, 1\n"
    "buffer_store_dwordx2 v[32:33], v23, s[28:31], 0 offen sc1\n"
    "s_mov_b64 exec, -1\n"
    "s_barrier\n"
    "s_waitcnt lgkmcnt(3)\n"
    "v_mfma_f32_16x16x32_bf16 v[128:131], a[32:35], a[48:51], v[128:131]\n"
    "ds_read_b128 a[0:3], v44 offset:49152\n"
    "v_mfma_f32_16x16x32_bf16 v[144:147], a[36:39], a[48:51], v[144:147]\n"
    "ds_read_b64_tr_b16 a[16:17], v52 offset:0\n"
    "buffer_load_dwordx4 v19, s[24:27], s38 offen offset:3072 lds\n"
    "v_mfma_f32_16x16x32_bf16 v[132:135], a[32:35], a[52:55], v[132:135]\n"
    "ds_read_b64_tr_b16 a[18:19], v52 offset:512\n"
    "v_mfma_f32_16x16x32_bf16 v[148:151], a[36:39], a[52:55], v[148:151]\n"
    "ds_read_b128 a[4:7], v44 offset:51200\n"
    "s_add_u32 m0, s36, 49152\n"
    "s_bfe_u32 s38, s58, 0x40010\n"
    "s_lshl_b32 s38, s38, 13\n"
    "s_or_b32 s38, s38, 128\n"
    "buffer_load_dwordx4 v12, s[20:23], s38 offen lds\n"
    "v_mfma_f32_16x16x32_bf16 v[160:163], a[40:43], a[48:51], v[160:163]\n"
    "ds_read_b64_tr_b16 a[20:21], v53 offset:0\n"
    "v_mfma_f32_16x16x32_bf16 v[164:167], a[40:43], a[52:55], v[164:167]\n"
    "ds_read_b64_tr_b16 a[22:23], v53 offset:512\n"
    "v_mfma_f32_16x16x32_bf16 v[136:139], a[32:35], a[56:59], v[136:139]\n"
    "ds_read_b128 a[8:11], v44 offset:53248\n"
    "v_mfma_f32_16x16x32_bf16 v[152:155], a[36:39], a[56:59], v[152:155]\n"
    "ds_read_b64_tr_b16 a[24:25], v54 offset:0\n"
    "buffer_load_dwordx4 v13, s[20:23], s38 offen offset:1024 lds\n"
    "v_mfma_f32_16x16x32_bf16 v[168:171], a[40:43], a[56:59], v[168:171]\n"
    "ds_read_b64_tr_b16 a[26:27], v54 offset:512\n"
    "s_waitcnt lgkmcnt(11)\n"
    "v_mfma_f32_16x16x32_bf16 v[176:179], a[44:47], a[48:51], v[176:179]\n"
    "ds_read_b128 a[12:15], v44 offset:55296\n"
    "v_mfma_f32_16x16x32_bf16 v[180:183], a[44:47], a[52:55], v[180:183]\n"
    "ds_read_b64_tr_b16 a[28:29], v55 offset:0\n"
    "v_mfma_f32_16x16x32_bf16 v[184:187], a[44:47], a[56:59], v[184:187]\n"
    "ds_read_b64_tr_b16 a[30:31], v55 offset:512\n"
    "buffer_load_dwordx4 v14, s[20:23], s38 offen offset:2048 lds\n"
    "s_waitcnt lgkmcnt(12)\n"
    "v_mfma_f32_16x16x32_bf16 v[140:143], a[32:35], a[60:63], v[140:143]\n"
    "v_mfma_f32_16x16x32_bf16 v[156:159], a[36:39], a[60:63], v[156:159]\n"
    "v_mfma_f32_16x16x32_bf16 v[172:175], a[40:43], a[60:63], v[172:175]\n"
    "v_mfma_f32_16x16x32_bf16 v[188:191], a[44:47], a[60:63], v[188:191]\n"
    "buffer_load_dwordx4 v15, s[20:23], s38 offen offset:3072 lds\n"
    "; ================ period 22 ================\n"
    "s_waitcnt lgkmcnt(3)\n"
    "v_mfma_f32_16x16x32_bf16 v[64:67], a[0:3], a[16:19], 0\n"
    "ds_read_b128 a[32:35], v45 offset:49152\n"
    "v_cvt_pk_bf16_f32 v56, v128, v129\n"
    "v_mfma_f32_16x16x32_bf16 v[80:83], a[4:7], a[16:19], 0\n"
    "ds_read_b64_tr_b16 a[48:49], v52 offset:4096\n"
    "v_cvt_pk_bf16_f32 v57, v130, v131\n"
    "v_mfma_f32_16x16x32_bf16 v[68:71], a[0:3], a[20:23], 0\n"
    "ds_read_b64_tr_b16 a[50:51], v52 offset:4608\n"
    "v_cvt_pk_bf16_f32 v58, v144, v145\n"
    "v_mfma_f32_16x16x32_bf16 v[84:87], a[4:7], a[20:23], 0\n"
    "ds_read_b128 a[36:39], v45 offset:51200\n"
    "s_add_u32 m0, s36, 65536\n"
    "s_bfe_u32 s38, s58, 0x40010\n"
    "s_lshl_b32 s38, s38, 18\n"
    "s_or_b32 s38, s38, 131072\n"
    "buffer_load_dwordx4 v16, s[24:27], s38 offen lds\n"
    "v_accvgpr_write_b32 a64, v56\n"
    "v_mfma_f32_16x16x32_bf16 v[96:99], a[8:11], a[16:19], 0\n"
    "ds_read_b64_tr_b16 a[52:53], v53 offset:4096\n"
    "v_cvt_pk_bf16_f32 v59, v146, v147\n"
    "v_mfma_f32_16x16x32_bf16 v[100:103], a[8:11], a[20:23], 0\n"
    "ds_read_b64_tr_b16 a[54:55], v53 offset:4608\n"
    "v_accvgpr_write_b32 a65, v57\n"
    "v_mfma_f32_16x16x32_bf16 v[72:75], a[0:3], a[24:27], 0\n"
    "ds_read_b128 a[40:43], v45 offset:53248\n"
    "v_cvt_pk_bf16_f32 v60, v132, v133\n"
    "v_mfma_f32_16x16x32_bf16 v[88:91], a[4:7], a[24:27], 0\n"
    "ds_read_b64_tr_b16 a[56:57], v54 offset:4096\n"
    "buffer_load_dwordx4 v17, s[24:27], s38 offen offset:1024 lds\n"
    "v_accvgpr_write_b32 a66, v58\n"
    "v_mfma_f32_16x16x32_bf16 v[104:107], a[8:11], a[24:27], 0\n"
    "ds_read_b64_tr_b16 a[58:59], v54 offset:4608\n"
    "v_cvt_pk_bf16_f32 v61, v134, v135\n"
    "s_waitcnt lgkmcnt(11)\n"
    "v_mfma_f32_16x16x32_bf16 v[112:115], a[12:15], a[16:19], 0\n"
    "ds_read_b128 a[44:47], v45 offset:55296\n"
    "v_accvgpr_write_b32 a67, v59\n"
    "v_mfma_f32_16x16x32_bf16 v[116:119], a[12:15], a[20:23], 0\n"
    "ds_read_b64_tr_b16 a[60:61], v55 offset:4096\n"
    "v_cvt_pk_bf16_f32 v62, v148, v149\n"
    "v_mfma_f32_16x16x32_bf16 v[120:123], a[12:15], a[24:27], 0\n"
    "ds_read_b64_tr_b16 a[62:63], v55 offset:4608\n"
    "buffer_load_dwordx4 v18, s[24:27], s38 offen offset:2048 lds\n"
    "v_accvgpr_write_b32 a68, v60\n"
    "s_waitcnt lgkmcnt(12)\n"
    "v_mfma_f32_16x16x32_bf16 v[76:79], a[0:3], a[28:31], 0\n"
    "v_cvt_pk_bf16_f32 v63, v150, v151\n"
    "v_mfma_f32_16x16x32_bf16 v[92:95], a[4:7], a[28:31], 0\n"
    "v_accvgpr_write_b32 a69, v61\n"
    "v_mfma_f32_16x16x32_bf16 v[108:111], a[8:11], a[28:31], 0\n"
    "v_cvt_pk_bf16_f32 v56, v136, v137\n"
    "v_mfma_f32_16x16x32_bf16 v[124:127], a[12:15], a[28:31], 0\n"
    "buffer_load_dwordx4 v19, s[24:27], s38 offen offset:3072 lds\n"
    "v_accvgpr_write_b32 a70, v62\n"
    "s_waitcnt vmcnt(17)\n"
    "s_barrier\n"
    "s_waitcnt lgkmcnt(3)\n"
    "v_mfma_f32_16x16x32_bf16 v[64:67], a[32:35], a[48:51], v[64:67]\n"
    "ds_read_b128 a[0:3], v46 offset:16384\n"
    "v_cvt_pk_bf16_f32 v57, v138, v139\n"
    "v_mfma_f32_16x16x32_bf16 v[80:83], a[36:39], a[48:51], v[80:83]\n"
    "ds_read_b64_tr_b16 a[16:17], v6 offset:0\n"
    "v_accvgpr_write_b32 a71, v63\n"
    "v_mfma_f32_16x16x32_bf16 v[68:71], a[32:35], a[52:55], v[68:71]\n"
    "ds_read_b64_tr_b16 a[18:19], v6 offset:512\n"
    "v_cvt_pk_bf16_f32 v58, v152, v153\n"
    "v_mfma_f32_16x16x32_bf16 v[84:87], a[36:39], a[52:55], v[84:87]\n"
    "ds_read_b128 a[4:7], v46 offset:18432\n"
    "s_add_u32 m0, s36, 81920\n"
    "s_bfe_u32 s38, s58, 0x40014\n"
    "s_lshl_b32 s38, s38, 13\n"
    "buffer_load_dwordx4 v12, s[20:23], s38 offen lds\n"
    "v_accvgpr_write_b32 a72, v56\n"
    "v_mfma_f32_16x16x32_bf16 v[96:99], a[40:43], a[48:51], v[96:99]\n"
    "ds_read_b64_tr_b16 a[20:21], v7 offset:0\n"
    "v_cvt_pk_bf16_f32 v59, v154, v155\n"
    "v_mfma_f32_16x16x32_bf16 v[100:103], a[40:43], a[52:55], v[100:103]\n"
    "ds_read_b64_tr_b16 a[22:23], v7 offset:512\n"
    "v_accvgpr_write_b32 a73, v57\n"
    "v_mfma_f32_16x16x32_bf16 v[72:75], a[32:35], a[56:59], v[72:75]\n"
    "ds_read_b128 a[8:11], v46 offset:20480\n"
    "v_cvt_pk_bf16_f32 v60, v160, v161\n"
    "v_mfma_f32_16x16x32_bf16 v[88:91], a[36:39], a[56:59], v[88:91]\n"
    "ds_read_b64_tr_b16 a[24:25], v8 offset:0\n"
    "buffer_load_dwordx4 v13, s[20:23], s38 offen offset:1024 lds\n"
    "v_accvgpr_write_b32 a74, v58\n"
    "v_mfma_f32_16x16x32_bf16 v[104:107], a[40:43], a[56:59], v[104:107]\n"
    "ds_read_b64_tr_b16 a[26:27], v8 offset:512\n"
    "v_cvt_pk_bf16_f32 v61, v162, v163\n"
    "s_waitcnt lgkmcnt(11)\n"
    "v_mfma_f32_16x16x32_bf16 v[112:115], a[44:47], a[48:51], v[112:115]\n"
    "ds_read_b128 a[12:15], v46 offset:22528\n"
    "v_accvgpr_write_b32 a75, v59\n"
    "v_mfma_f32_16x16x32_bf16 v[116:119], a[44:47], a[52:55], v[116:119]\n"
    "ds_read_b64_tr_b16 a[28:29], v9 offset:0\n"
    "v_cvt_pk_bf16_f32 v62, v176, v177\n"
    "v_mfma_f32_16x16x32_bf16 v[120:123], a[44:47], a[56:59], v[120:123]\n"
    "ds_read_b64_tr_b16 a[30:31], v9 offset:512\n"
    "buffer_load_dwordx4 v14, s[20:23], s38 offen offset:2048 lds\n"
    "v_accvgpr_write_b32 a80, v60\n"
    "s_waitcnt lgkmcnt(12)\n"
    "v_mfma_f32_16x16x32_bf16 v[76:79], a[32:35], a[60:63], v[76:79]\n"
    "v_cvt_pk_bf16_f32 v63, v178, v179\n"
    "v_mfma_f32_16x16x32_bf16 v[92:95], a[36:39], a[60:63], v[92:95]\n"
    "v_accvgpr_write_b32 a81, v61\n"
    "v_mfma_f32_16x16x32_bf16 v[108:111], a[40:43], a[60:63], v[108:111]\n"
    "v_cvt_pk_bf16_f32 v56, v164, v165\n"
    "v_mfma_f32_16x16x32_bf16 v[124:127], a[44:47], a[60:63], v[124:127]\n"
    "buffer_load_dwordx4 v15, s[20:23], s38 offen offset:3072 lds\n"
    "v_accvgpr_write_b32 a82, v62\n"
    "; ================ period 23 ================\n"
    "s_waitcnt lgkmcnt(3)\n"
    "v_mfma_f32_16x16x32_bf16 v[64:67], a[0:3], a[16:19], v[64:67]\n"
    "ds_read_b128 a[32:35], v47 offset:16384\n"
    "v_cvt_pk_bf16_f32 v57, v166, v167\n"
    "v_mfma_f32_16x16x32_bf16 v[80:83], a[4:7], a[16:19], v[80:83]\n"
    "ds_read_b64_tr_b16 a[48:49], v6 offset:4096\n"
    "v_accvgpr_write_b32 a83, v63\n"
    "v_mfma_f32_16x16x32_bf16 v[68:71], a[0:3], a[20:23], v[68:71]\n"
    "ds_read_b64_tr_b16 a[50:51], v6 offset:4608\n"
    "v_cvt_pk_bf16_f32 v58, v180, v181\n"
    "v_mfma_f32_16x16x32_bf16 v[84:87], a[4:7], a[20:23], v[84:87]\n"
    "ds_read_b128 a[36:39], v47 offset:18432\n"
    "s_add_u32 m0, s36, 98304\n"
    "s_bfe_u32 s38, s58, 0x40014\n"
    "s_lshl_b32 s38, s38, 18\n"
    "buffer_load_dwordx4 v16, s[24:27], s38 offen lds\n"
    "v_accvgpr_write_b32 a84, v56\n"
    "v_mfma_f32_16x16x32_bf16 v[96:99], a[8:11], a[16:19], v[96:99]\n"
    "ds_read_b64_tr_b16 a[52:53], v7 offset:4096\n"
    "v_cvt_pk_bf16_f32 v59, v182, v183\n"
    "v_mfma_f32_16x16x32_bf16 v[100:103], a[8:11], a[20:23], v[100:103]\n"
    "ds_read_b64_tr_b16 a[54:55], v7 offset:4608\n"
    "v_accvgpr_write_b32 a85, v57\n"
    "v_mfma_f32_16x16x32_bf16 v[72:75], a[0:3], a[24:27], v[72:75]\n"
    "ds_read_b128 a[40:43], v47 offset:20480\n"
    "v_cvt_pk_bf16_f32 v60, v168, v169\n"
    "v_mfma_f32_16x16x32_bf16 v[88:91], a[4:7], a[24:27], v[88:91]\n"
    "ds_read_b64_tr_b16 a[56:57], v8 offset:4096\n"
    "buffer_load_dwordx4 v17, s[24:27], s38 offen offset:1024 lds\n"
    "v_accvgpr_write_b32 a86, v58\n"
    "v_mfma_f32_16x16x32_bf16 v[104:107], a[8:11], a[24:27], v[104:107]\n"
    "ds_read_b64_tr_b16 a[58:59], v8 offset:4608\n"
    "v_cvt_pk_bf16_f32 v61, v170, v171\n"
    "s_waitcnt lgkmcnt(11)\n"
    "v_mfma_f32_16x16x32_bf16 v[112:115], a[12:15], a[16:19], v[112:115]\n"
    "ds_read_b128 a[44:47], v47 offset:22528\n"
    "v_accvgpr_write_b32 a87, v59\n"
    "v_mfma_f32_16x16x32_bf16 v[116:119], a[12:15], a[20:23], v[116:119]\n"
    "ds_read_b64_tr_b16 a[60:61], v9 offset:4096\n"
    "v_cvt_pk_bf16_f32 v62, v184, v185\n"
    "v_mfma_f32_16x16x32_bf16 v[120:123], a[12:15], a[24:27], v[120:123]\n"
    "ds_read_b64_tr_b16 a[62:63], v9 offset:4608\n"
    "buffer_load_dwordx4 v18, s[24:27], s38 offen offset:2048 lds\n"
    "v_accvgpr_write_b32 a88, v60\n"
    "s_waitcnt lgkmcnt(12)\n"
    "v_mfma_f32_16x16x32_bf16 v[76:79], a[0:3], a[28:31], v[76:79]\n"
    "v_cvt_pk_bf16_f32 v63, v186, v187\n"
    "v_mfma_f32_16x16x32_bf16 v[92:95], a[4:7], a[28:31], v[92:95]\n"
    "v_accvgpr_write_b32 a89, v61\n"
    "v_mfma_f32_16x16x32_bf16 v[108:111], a[8:11], a[28:31], v[108:111]\n"
    "v_cvt_pk_bf16_f32 v56, v140, v141\n"
    "v_mfma_f32_16x16x32_bf16 v[124:127], a[12:15], a[28:31], v[124:127]\n"
    "buffer_load_dwordx4 v19, s[24:27], s38 offen offset:3072 lds\n"
    "v_accvgpr_write_b32 a90, v62\n"
    "s_waitcnt vmcnt(16)\n"
    "s_barrier\n"
    "s_waitcnt lgkmcnt(3)\n"
    "v_mfma_f32_16x16x32_bf16 v[64:67], a[32:35], a[48:51], v[64:67]\n"
    "ds_read_b128 a[0:3], v4 offset:16384\n"
    "v_cvt_pk_bf16_f32 v57, v142, v143\n"
    "v_mfma_f32_16x16x32_bf16 v[80:83], a[36:39], a[48:51], v[80:83]\n"
    "ds_read_b64_tr_b16 a[16:17], v6 offset:32768\n"
    "v_accvgpr_write_b32 a91, v63\n"
    "v_mfma_f32_16x16x32_bf16 v[68:71], a[32:35], a[52:55], v[68:71]\n"
    "ds_read_b64_tr_b16 a[18:19], v6 offset:33280\n"
    "v_cvt_pk_bf16_f32 v58, v156, v157\n"
    "v_mfma_f32_16x16x32_bf16 v[84:87], a[36:39], a[52:55], v[84:87]\n"
    "ds_read_b128 a[4:7], v4 offset:18432\n"
    "s_add_u32 m0, s36, 114688\n"
    "s_bfe_u32 s38, s58, 0x40014\n"
    "s_lshl_b32 s38, s38, 13\n"
    "s_or_b32 s38, s38, 128\n"
    "buffer_load_dwordx4 v12, s[20:23], s38 offen lds\n"
    "v_accvgpr_write_b32 a76, v56\n"
    "v_mfma_f32_16x16x32_bf16 v[96:99], a[40:43], a[48:51], v[96:99]\n"
    "ds_read_b64_tr_b16 a[20:21], v7 offset:32768\n"
    "v_cvt_pk_bf16_f32 v59, v158, v159\n"
    "v_mfma_f32_16x16x32_bf16 v[100:103], a[40:43], a[52:55], v[100:103]\n"
    "ds_read_b64_tr_b16 a[22:23], v7 offset:33280\n"
    "v_accvgpr_write_b32 a77, v57\n"
    "v_mfma_f32_16x16x32_bf16 v[72:75], a[32:35], a[56:59], v[72:75]\n"
    "ds_read_b128 a[8:11], v4 offset:20480\n"
    "v_cvt_pk_bf16_f32 v60, v172, v173\n"
    "v_mfma_f32_16x16x32_bf16 v[88:91], a[36:39], a[56:59], v[88:91]\n"
    "ds_read_b64_tr_b16 a[24:25], v8 offset:32768\n"
    "buffer_load_dwordx4 v13, s[20:23], s38 offen offset:1024 lds\n"
    "v_accvgpr_write_b32 a78, v58\n"
    "v_mfma_f32_16x16x32_bf16 v[104:107], a[40:43], a[56:59], v[104:107]\n"
    "ds_read_b64_tr_b16 a[26:27], v8 offset:33280\n"
    "v_cvt_pk_bf16_f32 v61, v174, v175\n"
    "s_waitcnt lgkmcnt(11)\n"
    "v_mfma_f32_16x16x32_bf16 v[112:115], a[44:47], a[48:51], v[112:115]\n"
    "ds_read_b128 a[12:15], v4 offset:22528\n"
    "v_accvgpr_write_b32 a79, v59\n"
    "v_mfma_f32_16x16x32_bf16 v[116:119], a[44:47], a[52:55], v[116:119]\n"
    "ds_read_b64_tr_b16 a[28:29], v9 offset:32768\n"
    "v_cvt_pk_bf16_f32 v62, v188, v189\n"
    "v_mfma_f32_16x16x32_bf16 v[120:123], a[44:47], a[56:59], v[120:123]\n"
    "ds_read_b64_tr_b16 a[30:31], v9 offset:33280\n"
    "buffer_load_dwordx4 v14, s[20:23], s38 offen offset:2048 lds\n"
    "v_accvgpr_write_b32 a92, v60\n"
    "s_waitcnt lgkmcnt(12)\n"
    "v_mfma_f32_16x16x32_bf16 v[76:79], a[32:35], a[60:63], v[76:79]\n"
    "v_cvt_pk_bf16_f32 v63, v190, v191\n"
    "v_mfma_f32_16x16x32_bf16 v[92:95], a[36:39], a[60:63], v[92:95]\n"
    "v_accvgpr_write_b32 a93, v61\n"
    "v_mfma_f32_16x16x32_bf16 v[108:111], a[40:43], a[60:63], v[108:111]\n"
    "v_accvgpr_write_b32 a94, v62\n"
    "v_mfma_f32_16x16x32_bf16 v[124:127], a[44:47], a[60:63], v[124:127]\n"
    "buffer_load_dwordx4 v15, s[20:23], s38 offen offset:3072 lds\n"
    "v_accvgpr_write_b32 a95, v63\n"
    "; ================ period 24 ================\n"
    "s_waitcnt lgkmcnt(3)\n"
    "v_mfma_f32_16x16x32_bf16 v[128:131], a[0:3], a[16:19], 0\n"
    "ds_read_b128 a[32:35], v5 offset:16384\n"
    "v_cvt_pk_bf16_f32 v56, v64, v65\n"
    "v_mfma_f32_16x16x32_bf16 v[144:147], a[4:7], a[16:19], 0\n"
    "ds_read_b64_tr_b16 a[48:49], v6 offset:36864\n"
    "v_cvt_pk_bf16_f32 v57, v66, v67\n"
    "v_mfma_f32_16x16x32_bf16 v[132:135], a[0:3], a[20:23], 0\n"
    "ds_read_b64_tr_b16 a[50:51], v6 offset:37376\n"
    "v_cvt_pk_bf16_f32 v58, v80, v81\n"
    "v_mfma_f32_16x16x32_bf16 v[148:151], a[4:7], a[20:23], 0\n"
    "ds_read_b128 a[36:39], v5 offset:18432\n"
    "s_add_u32 m0, s36, 131072\n"
    "s_bfe_u32 s38, s58, 0x40014\n"
    "s_lshl_b32 s38, s38, 18\n"
    "s_or_b32 s38, s38, 131072\n"
    "buffer_load_dwordx4 v16, s[24:27], s38 offen lds\n"
    "v_accvgpr_write_b32 a96, v56\n"
    "v_mfma_f32_16x16x32_bf16 v[160:163], a[8:11], a[16:19], 0\n"
    "ds_read_b64_tr_b16 a[52:53], v7 offset:36864\n"
    "v_cvt_pk_bf16_f32 v59, v82, v83\n"
    "v_mfma_f32_16x16x32_bf16 v[164:167], a[8:11], a[20:23], 0\n"
    "ds_read_b64_tr_b16 a[54:55], v7 offset:37376\n"
    "v_accvgpr_write_b32 a97, v57\n"
    "v_mfma_f32_16x16x32_bf16 v[136:139], a[0:3], a[24:27], 0\n"
    "ds_read_b128 a[40:43], v5 offset:20480\n"
    "v_cvt_pk_bf16_f32 v60, v68, v69\n"
    "v_mfma_f32_16x16x32_bf16 v[152:155], a[4:7], a[24:27], 0\n"
    "ds_read_b64_tr_b16 a[56:57], v8 offset:36864\n"
    "buffer_load_dwordx4 v17, s[24:27], s38 offen offset:1024 lds\n"
    "v_accvgpr_write_b32 a98, v58\n"
    "v_mfma_f32_16x16x32_bf16 v[168:171], a[8:11], a[24:27], 0\n"
    "ds_read_b64_tr_b16 a[58:59], v8 offset:37376\n"
    "v_cvt_pk_bf16_f32 v61, v70, v71\n"
    "s_waitcnt lgkmcnt(11)\n"
    "v_mfma_f32_16x16x32_bf16 v[176:179], a[12:15], a[16:19], 0\n"
    "ds_read_b128 a[44:47], v5 offset:22528\n"
    "v_accvgpr_write_b32 a99, v59\n"
    "v_mfma_f32_16x16x32_bf16 v[180:183], a[12:15], a[20:23], 0\n"
    "ds_read_b64_tr_b16 a[60:61], v9 offset:36864\n"
    "v_cvt_pk_bf16_f32 v62, v84, v85\n"
    "v_mfma_f32_16x16x32_bf16 v[184:187], a[12:15], a[24:27], 0\n"
    "ds_read_b64_tr_b16 a[62:63], v9 offset:37376\n"
    "buffer_load_dwordx4 v18, s[24:27], s38 offen offset:2048 lds\n"
    "v_accvgpr_write_b32 a100, v60\n"
    "s_waitcnt lgkmcnt(12)\n"
    "v_mfma_f32_16x16x32_bf16 v[140:143], a[0:3], a[28:31], 0\n"
    "v_cvt_pk_bf16_f32 v63, v86, v87\n"
    "v_mfma_f32_16x16x32_bf16 v[156:159], a[4:7], a[28:31], 0\n"
    "v_accvgpr_write_b32 a101, v61\n"
    "v_mfma_f32_16x16x32_bf16 v[172:175], a[8:11], a[28:31], 0\n"
    "v_cvt_pk_bf16_f32 v56, v72, v73\n"
    "v_mfma_f32_16x16x32_bf16 v[188:191], a[12:15], a[28:31], 0\n"
    "buffer_load_dwordx4 v19, s[24:27], s38 offen offset:3072 lds\n"
    "v_accvgpr_write_b32 a102, v62\n"
    "s_waitcnt vmcnt(16)\n"
    "s_barrier\n"
    "s_waitcnt lgkmcnt(3)\n"
    "v_mfma_f32_16x16x32_bf16 v[128:131], a[32:35], a[48:51], v[128:131]\n"
    "ds_read_b128 a[0:3], v4 offset:49152\n"
    "v_cvt_pk_bf16_f32 v57, v74, v75\n"
    "v_mfma_f32_16x16x32_bf16 v[144:147], a[36:39], a[48:51], v[144:147]\n"
    "ds_read_b64_tr_b16 a[16:17], v48 offset:0\n"
    "v_accvgpr_write_b32 a103, v63\n"
    "v_mfma_f32_16x16x32_bf16 v[132:135], a[32:35], a[52:55], v[132:135]\n"
    "ds_read_b64_tr_b16 a[18:19], v48 offset:512\n"
    "v_cvt_pk_bf16_f32 v58, v88, v89\n"
    "v_mfma_f32_16x16x32_bf16 v[148:151], a[36:39], a[52:55], v[148:151]\n"
    "ds_read_b128 a[4:7], v4 offset:51200\n"
    "s_add_u32 m0, s36, 147456\n"
    "s_bfe_u32 s38, s58, 0x40018\n"
    "s_lshl_b32 s38, s38, 13\n"
    "buffer_load_dwordx4 v12, s[20:23], s38 offen lds\n"
    "v_accvgpr_write_b32 a104, v56\n"
    "v_mfma_f32_16x16x32_bf16 v[160:163], a[40:43], a[48:51], v[160:163]\n"
    "ds_read_b64_tr_b16 a[20:21], v49 offset:0\n"
    "v_cvt_pk_bf16_f32 v59, v90, v91\n"
    "v_mfma_f32_16x16x32_bf16 v[164:167], a[40:43], a[52:55], v[164:167]\n"
    "ds_read_b64_tr_b16 a[22:23], v49 offset:512\n"
    "v_accvgpr_write_b32 a105, v57\n"
    "v_mfma_f32_16x16x32_bf16 v[136:139], a[32:35], a[56:59], v[136:139]\n"
    "ds_read_b128 a[8:11], v4 offset:53248\n"
    "v_cvt_pk_bf16_f32 v60, v96, v97\n"
    "v_mfma_f32_16x16x32_bf16 v[152:155], a[36:39], a[56:59], v[152:155]\n"
    "ds_read_b64_tr_b16 a[24:25], v50 offset:0\n"
    "buffer_load_dwordx4 v13, s[20:23], s38 offen offset:1024 lds\n"
    "v_accvgpr_write_b32 a106, v58\n"
    "v_mfma_f32_16x16x32_bf16 v[168:171], a[40:43], a[56:59], v[168:171]\n"
    "ds_read_b64_tr_b16 a[26:27], v50 offset:512\n"
    "v_cvt_pk_bf16_f32 v61, v98, v99\n"
    "s_waitcnt lgkmcnt(11)\n"
    "v_mfma_f32_16x16x32_bf16 v[176:179], a[44:47], a[48:51], v[176:179]\n"
    "ds_read_b128 a[12:15], v4 offset:55296\n"
    "v_accvgpr_write_b32 a107, v59\n"
    "v_mfma_f32_16x16x32_bf16 v[180:183], a[44:47], a[52:55], v[180:183]\n"
    "ds_read_b64_tr_b16 a[28:29], v51 offset:0\n"
    "v_cvt_pk_bf16_f32 v62, v112, v113\n"
    "v_mfma_f32_16x16x32_bf16 v[184:187], a[44:47], a[56:59], v[184:187]\n"
    "ds_read_b64_tr_b16 a[30:31], v51 offset:512\n"
    "buffer_load_dwordx4 v14, s[20:23], s38 offen offset:2048 lds\n"
    "v_accvgpr_write_b32 a112, v60\n"
    "s_waitcnt lgkmcnt(12)\n"
    "v_mfma_f32_16x16x32_bf16 v[140:143], a[32:35], a[60:63], v[140:143]\n"
    "v_cvt_pk_bf16_f32 v63, v114, v115\n"
    "v_mfma_f32_16x16x32_bf16 v[156:159], a[36:39], a[60:63], v[156:159]\n"
    "v_accvgpr_write_b32 a113, v61\n"
    "v_mfma_f32_16x16x32_bf16 v[172:175], a[40:43], a[60:63], v[172:175]\n"
    "v_cvt_pk_bf16_f32 v56, v100, v101\n"
    "v_mfma_f32_16x16x32_bf16 v[188:191], a[44:47], a[60:63], v[188:191]\n"
    "buffer_load_dwordx4 v15, s[20:23], s38 offen offset:3072 lds\n"
    "v_accvgpr_write_b32 a114, v62\n"
    "; ================ period 25 ================\n"
    "s_waitcnt lgkmcnt(3)\n"
    "v_mfma_f32_16x16x32_bf16 v[128:131], a[0:3], a[16:19], v[128:131]\n"
    "ds_read_b128 a[32:35], v5 offset:49152\n"
    "v_cvt_pk_bf16_f32 v57, v102, v103\n"
    "v_mfma_f32_16x16x32_bf16 v[144:147], a[4:7], a[16:19], v[144:147]\n"
    "ds_read_b64_tr_b16 a[48:49], v48 offset:4096\n"
    "v_accvgpr_write_b32 a115, v63\n"
    "v_mfma_f32_16x16x32_bf16 v[132:135], a[0:3], a[20:23], v[132:135]\n"
    "ds_read_b64_tr_b16 a[50:51], v48 offset:4608\n"
    "v_cvt_pk_bf16_f32 v58, v116, v117\n"
    "v_mfma_f32_16x16x32_bf16 v[148:151], a[4:7], a[20:23], v[148:151]\n"
    "ds_read_b128 a[36:39], v5 offset:51200\n"
    "s_add_u32 m0, s36, 0\n"
    "s_bfe_u32 s38, s58, 0x40018\n"
    "s_lshl_b32 s38, s38, 18\n"
    "buffer_load_dwordx4 v16, s[24:27], s38 offen lds\n"
    "v_accvgpr_write_b32 a116, v56\n"
    "v_mfma_f32_16x16x32_bf16 v[160:163], a[8:11], a[16:19], v[160:163]\n"
    "ds_read_b64_tr_b16 a[52:53], v49 offset:4096\n"
    "v_cvt_pk_bf16_f32 v59, v118, v119\n"
    "v_mfma_f32_16x16x32_bf16 v[164:167], a[8:11], a[20:23], v[164:167]\n"
    "ds_read_b64_tr_b16 a[54:55], v49 offset:4608\n"
    "v_accvgpr_write_b32 a117, v57\n"
    "v_mfma_f32_16x16x32_bf16 v[136:139], a[0:3], a[24:27], v[136:139]\n"
    "ds_read_b128 a[40:43], v5 offset:53248\n"
    "v_cvt_pk_bf16_f32 v60, v104, v105\n"
    "v_mfma_f32_16x16x32_bf16 v[152:155], a[4:7], a[24:27], v[152:155]\n"
    "ds_read_b64_tr_b16 a[56:57], v50 offset:4096\n"
    "buffer_load_dwordx4 v17, s[24:27], s38 offen offset:1024 lds\n"
    "v_accvgpr_write_b32 a118, v58\n"
    "v_mfma_f32_16x16x32_bf16 v[168:171], a[8:11], a[24:27], v[168:171]\n"
    "ds_read_b64_tr_b16 a[58:59], v50 offset:4608\n"
    "v_cvt_pk_bf16_f32 v61, v106, v107\n"
    "s_waitcnt lgkmcnt(11)\n"
    "v_mfma_f32_16x16x32_bf16 v[176:179], a[12:15], a[16:19], v[176:179]\n"
    "ds_read_b128 a[44:47], v5 offset:55296\n"
    "v_accvgpr_write_b32 a119, v59\n"
    "v_mfma_f32_16x16x32_bf16 v[180:183], a[12:15], a[20:23], v[180:183]\n"
    "ds_read_b64_tr_b16 a[60:61], v51 offset:4096\n"
    "v_cvt_pk_bf16_f32 v62, v120, v121\n"
    "v_mfma_f32_16x16x32_bf16 v[184:187], a[12:15], a[24:27], v[184:187]\n"
    "ds_read_b64_tr_b16 a[62:63], v51 offset:4608\n"
    "buffer_load_dwordx4 v18, s[24:27], s38 offen offset:2048 lds\n"
    "v_accvgpr_write_b32 a120, v60\n"
    "s_waitcnt lgkmcnt(12)\n"
    "v_mfma_f32_16x16x32_bf16 v[140:143], a[0:3], a[28:31], v[140:143]\n"
    "v_cvt_pk_bf16_f32 v63, v122, v123\n"
    "v_mfma_f32_16x16x32_bf16 v[156:159], a[4:7], a[28:31], v[156:159]\n"
    "v_accvgpr_write_b32 a121, v61\n"
    "v_mfma_f32_16x16x32_bf16 v[172:175], a[8:11], a[28:31], v[172:175]\n"
    "v_cvt_pk_bf16_f32 v56, v76, v77\n"
    "v_mfma_f32_16x16x32_bf16 v[188:191], a[12:15], a[28:31], v[188:191]\n"
    "buffer_load_dwordx4 v19, s[24:27], s38 offen offset:3072 lds\n"
    "v_accvgpr_write_b32 a122, v62\n"
    "s_waitcnt vmcnt(16)\n"
    "s_barrier\n"
    "s_waitcnt lgkmcnt(3)\n"
    "v_mfma_f32_16x16x32_bf16 v[128:131], a[32:35], a[48:51], v[128:131]\n"
    "ds_read_b128 a[0:3], v44 offset:16384\n"
    "v_cvt_pk_bf16_f32 v57, v78, v79\n"
    "v_mfma_f32_16x16x32_bf16 v[144:147], a[36:39], a[48:51], v[144:147]\n"
    "ds_read_b64_tr_b16 a[16:17], v48 offset:32768\n"
    "v_accvgpr_write_b32 a123, v63\n"
    "v_mfma_f32_16x16x32_bf16 v[132:135], a[32:35], a[52:55], v[132:135]\n"
    "ds_read_b64_tr_b16 a[18:19], v48 offset:33280\n"
    "v_cvt_pk_bf16_f32 v58, v92, v93\n"
    "v_mfma_f32_16x16x32_bf16 v[148:151], a[36:39], a[52:55], v[148:151]\n"
    "ds_read_b128 a[4:7], v44 offset:18432\n"
    "s_add_u32 m0, s36, 16384\n"
    "s_bfe_u32 s38, s58, 0x40018\n"
    "s_lshl_b32 s38, s38, 13\n"
    "s_or_b32 s38, s38, 128\n"
    "buffer_load_dwordx4 v12, s[20:23], s38 offen lds\n"
    "v_accvgpr_write_b32 a108, v56\n"
    "v_mfma_f32_16x16x32_bf16 v[160:163], a[40:43], a[48:51], v[160:163]\n"
    "ds_read_b64_tr_b16 a[20:21], v49 offset:32768\n"
    "v_cvt_pk_bf16_f32 v59, v94, v95\n"
    "v_mfma_f32_16x16x32_bf16 v[164:167], a[40:43], a[52:55], v[164:167]\n"
    "ds_read_b64_tr_b16 a[22:23], v49 offset:33280\n"
    "v_accvgpr_write_b32 a109, v57\n"
    "v_mfma_f32_16x16x32_bf16 v[136:139], a[32:35], a[56:59], v[136:139]\n"
    "ds_read_b128 a[8:11], v44 offset:20480\n"
    "v_cvt_pk_bf16_f32 v60, v108, v109\n"
    "v_mfma_f32_16x16x32_bf16 v[152:155], a[36:39], a[56:59], v[152:155]\n"
    "ds_read_b64_tr_b16 a[24:25], v50 offset:32768\n"
    "buffer_load_dwordx4 v13, s[20:23], s38 offen offset:1024 lds\n"
    "v_accvgpr_write_b32 a110, v58\n"
    "v_mfma_f32_16x16x32_bf16 v[168:171], a[40:43], a[56:59], v[168:171]\n"
    "ds_read_b64_tr_b16 a[26:27], v50 offset:33280\n"
    "v_cvt_pk_bf16_f32 v61, v110, v111\n"
    "s_waitcnt lgkmcnt(11)\n"
    "v_mfma_f32_16x16x32_bf16 v[176:179], a[44:47], a[48:51], v[176:179]\n"
    "ds_read_b128 a[12:15], v44 offset:22528\n"
    "v_accvgpr_write_b32 a111, v59\n"
    "v_mfma_f32_16x16x32_bf16 v[180:183], a[44:47], a[52:55], v[180:183]\n"
    "ds_read_b64_tr_b16 a[28:29], v51 offset:32768\n"
    "v_cvt_pk_bf16_f32 v62, v124, v125\n"
    "v_mfma_f32_16x16x32_bf16 v[184:187], a[44:47], a[56:59], v[184:187]\n"
    "ds_read_b64_tr_b16 a[30:31], v51 offset:33280\n"
    "buffer_load_dwordx4 v14, s[20:23], s38 offen offset:2048 lds\n"
    "v_accvgpr_write_b32 a124, v60\n"
    "s_waitcnt lgkmcnt(12)\n"
    "v_mfma_f32_16x16x32_bf16 v[140:143], a[32:35], a[60:63], v[140:143]\n"
    "v_cvt_pk_bf16_f32 v63, v126, v127\n"
    "v_mfma_f32_16x16x32_bf16 v[156:159], a[36:39], a[60:63], v[156:159]\n"
    "v_accvgpr_write_b32 a125, v61\n"
    "v_mfma_f32_16x16x32_bf16 v[172:175], a[40:43], a[60:63], v[172:175]\n"
    "v_accvgpr_write_b32 a126, v62\n"
    "v_mfma_f32_16x16x32_bf16 v[188:191], a[44:47], a[60:63], v[188:191]\n"
    "buffer_load_dwordx4 v15, s[20:23], s38 offen offset:3072 lds\n"
    "v_accvgpr_write_b32 a127, v63\n"
    "; ================ period 26 ================\n"
    "s_waitcnt lgkmcnt(3)\n"
    "v_mfma_f32_16x16x32_bf16 v[64:67], a[0:3], a[16:19], 0\n"
    "ds_read_b128 a[32:35], v45 offset:16384\n"
    "v_cvt_pk_bf16_f32 v56, v128, v129\n"
    "v_mfma_f32_16x16x32_bf16 v[80:83], a[4:7], a[16:19], 0\n"
    "ds_read_b64_tr_b16 a[48:49], v48 offset:36864\n"
    "v_cvt_pk_bf16_f32 v57, v130, v131\n"
    "v_mfma_f32_16x16x32_bf16 v[68:71], a[0:3], a[20:23], 0\n"
    "ds_read_b64_tr_b16 a[50:51], v48 offset:37376\n"
    "v_cvt_pk_bf16_f32 v58, v144, v145\n"
    "v_mfma_f32_16x16x32_bf16 v[84:87], a[4:7], a[20:23], 0\n"
    "ds_read_b128 a[36:39], v45 offset:18432\n"
    "s_add_u32 m0, s36, 32768\n"
    "s_bfe_u32 s38, s58, 0x40018\n"
    "s_lshl_b32 s38, s38, 18\n"
    "s_or_b32 s38, s38, 131072\n"
    "buffer_load_dwordx4 v16, s[24:27], s38 offen lds\n"
    "v_accvgpr_write_b32 a128, v56\n"
    "v_mfma_f32_16x16x32_bf16 v[96:99], a[8:11], a[16:19], 0\n"
    "ds_read_b64_tr_b16 a[52:53], v49 offset:36864\n"
    "v_cvt_pk_bf16_f32 v59, v146, v147\n"
    "v_mfma_f32_16x16x32_bf16 v[100:103], a[8:11], a[20:23], 0\n"
    "ds_read_b64_tr_b16 a[54:55], v49 offset:37376\n"
    "v_accvgpr_write_b32 a129, v57\n"
    "v_mfma_f32_16x16x32_bf16 v[72:75], a[0:3], a[24:27], 0\n"
    "ds_read_b128 a[40:43], v45 offset:20480\n"
    "v_cvt_pk_bf16_f32 v60, v132, v133\n"
    "v_mfma_f32_16x16x32_bf16 v[88:91], a[4:7], a[24:27], 0\n"
    "ds_read_b64_tr_b16 a[56:57], v50 offset:36864\n"
    "buffer_load_dwordx4 v17, s[24:27], s38 offen offset:1024 lds\n"
    "v_accvgpr_write_b32 a130, v58\n"
    "v_mfma_f32_16x16x32_bf16 v[104:107], a[8:11], a[24:27], 0\n"
    "ds_read_b64_tr_b16 a[58:59], v50 offset:37376\n"
    "v_cvt_pk_bf16_f32 v61, v134, v135\n"
    "s_waitcnt lgkmcnt(11)\n"
    "v_mfma_f32_16x16x32_bf16 v[112:115], a[12:15], a[16:19], 0\n"
    "ds_read_b128 a[44:47], v45 offset:22528\n"
    "v_accvgpr_write_b32 a131, v59\n"
    "v_mfma_f32_16x16x32_bf16 v[116:119], a[12:15], a[20:23], 0\n"
    "ds_read_b64_tr_b16 a[60:61], v51 offset:36864\n"
    "v_cvt_pk_bf16_f32 v62, v148, v149\n"
    "v_mfma_f32_16x16x32_bf16 v[120:123], a[12:15], a[24:27], 0\n"
    "ds_read_b64_tr_b16 a[62:63], v51 offset:37376\n"
    "buffer_load_dwordx4 v18, s[24:27], s38 offen offset:2048 lds\n"
    "v_accvgpr_write_b32 a132, v60\n"
    "s_waitcnt lgkmcnt(12)\n"
    "v_mfma_f32_16x16x32_bf16 v[76:79], a[0:3], a[28:31], 0\n"
    "v_cvt_pk_bf16_f32 v63, v150, v151\n"
    "v_mfma_f32_16x16x32_bf16 v[92:95], a[4:7], a[28:31], 0\n"
    "v_accvgpr_write_b32 a133, v61\n"
    "v_mfma_f32_16x16x32_bf16 v[108:111], a[8:11], a[28:31], 0\n"
    "v_cvt_pk_bf16_f32 v56, v136, v137\n"
    "v_mfma_f32_16x16x32_bf16 v[124:127], a[12:15], a[28:31], 0\n"
    "buffer_load_dwordx4 v19, s[24:27], s38 offen offset:3072 lds\n"
    "v_accvgpr_write_b32 a134, v62\n"
    "s_waitcnt vmcnt(16)\n"
    "s_barrier\n"
    "buffer_load_dwordx2 v[36:37], v22, s[28:31], 0 offen sc1\n"
    "s_waitcnt lgkmcnt(3)\n"
    "v_mfma_f32_16x16x32_bf16 v[64:67], a[32:35], a[48:51], v[64:67]\n"
    "ds_read_b128 a[0:3], v44 offset:49152\n"
    "v_cvt_pk_bf16_f32 v57, v138, v139\n"
    "v_mfma_f32_16x16x32_bf16 v[80:83], a[36:39], a[48:51], v[80:83]\n"
    "ds_read_b64_tr_b16 a[16:17], v52 offset:0\n"
    "v_accvgpr_write_b32 a135, v63\n"
    "v_mfma_f32_16x16x32_bf16 v[68:71], a[32:35], a[52:55], v[68:71]\n"
    "ds_read_b64_tr_b16 a[18:19], v52 offset:512\n"
    "v_cvt_pk_bf16_f32 v58, v152, v153\n"
    "v_mfma_f32_16x16x32_bf16 v[84:87], a[36:39], a[52:55], v[84:87]\n"
    "ds_read_b128 a[4:7], v44 offset:51200\n"
    "s_add_u32 m0, s36, 49152\n"
    "s_bfe_u32 s38, s58, 0x4001c\n"
    "s_lshl_b32 s38, s38, 13\n"
    "buffer_load_dwordx4 v12, s[20:23], s38 offen lds\n"
    "v_accvgpr_write_b32 a136, v56\n"
    "v_mfma_f32_16x16x32_bf16 v[96:99], a[40:43], a[48:51], v[96:99]\n"
    "ds_read_b64_tr_b16 a[20:21], v53 offset:0\n"
    "v_cvt_pk_bf16_f32 v59, v154, v155\n"
    "v_mfma_f32_16x16x32_bf16 v[100:103], a[40:43], a[52:55], v[100:103]\n"
    "ds_read_b64_tr_b16 a[22:23], v53 offset:512\n"
    "v_accvgpr_write_b32 a137, v57\n"
    "v_mfma_f32_16x16x32_bf16 v[72:75], a[32:35], a[56:59], v[72:75]\n"
    "ds_read_b128 a[8:11], v44 offset:53248\n"
    "v_cvt_pk_bf16_f32 v60, v160, v161\n"
    "v_mfma_f32_16x16x32_bf16 v[88:91], a[36:39], a[56:59], v[88:91]\n"
    "ds_read_b64_tr_b16 a[24:25], v54 offset:0\n"
    "buffer_load_dwordx4 v13, s[20:23], s38 offen offset:1024 lds\n"
    "v_accvgpr_write_b32 a138, v58\n"
    "v_mfma_f32_16x16x32_bf16 v[104:107], a[40:43], a[56:59], v[104:107]\n"
    "ds_read_b64_tr_b16 a[26:27], v54 offset:512\n"
    "v_cvt_pk_bf16_f32 v61, v162, v163\n"
    "s_waitcnt lgkmcnt(11)\n"
    "v_mfma_f32_16x16x32_bf16 v[112:115], a[44:47], a[48:51], v[112:115]\n"
    "ds_read_b128 a[12:15], v44 offset:55296\n"
    "v_accvgpr_write_b32 a139, v59\n"
    "v_mfma_f32_16x16x32_bf16 v[116:119], a[44:47], a[52:55], v[116:119]\n"
    "ds_read_b64_tr_b16 a[28:29], v55 offset:0\n"
    "v_cvt_pk_bf16_f32 v62, v176, v177\n"
    "v_mfma_f32_16x16x32_bf16 v[120:123], a[44:47], a[56:59], v[120:123]\n"
    "ds_read_b64_tr_b16 a[30:31], v55 offset:512\n"
    "buffer_load_dwordx4 v14, s[20:23], s38 offen offset:2048 lds\n"
    "v_accvgpr_write_b32 a144, v60\n"
    "s_waitcnt lgkmcnt(12)\n"
    "v_mfma_f32_16x16x32_bf16 v[76:79], a[32:35], a[60:63], v[76:79]\n"
    "v_cvt_pk_bf16_f32 v63, v178, v179\n"
    "v_mfma_f32_16x16x32_bf16 v[92:95], a[36:39], a[60:63], v[92:95]\n"
    "v_accvgpr_write_b32 a145, v61\n"
    "v_mfma_f32_16x16x32_bf16 v[108:111], a[40:43], a[60:63], v[108:111]\n"
    "v_cvt_pk_bf16_f32 v56, v164, v165\n"
    "v_mfma_f32_16x16x32_bf16 v[124:127], a[44:47], a[60:63], v[124:127]\n"
    "buffer_load_dwordx4 v15, s[20:23], s38 offen offset:3072 lds\n"
    "v_accvgpr_write_b32 a146, v62\n"
    "; ================ period 27 ================\n"
    "s_waitcnt lgkmcnt(3)\n"
    "v_mfma_f32_16x16x32_bf16 v[64:67], a[0:3], a[16:19], v[64:67]\n"
    "ds_read_b128 a[32:35], v45 offset:49152\n"
    "v_cvt_pk_bf16_f32 v57, v166, v167\n"
    "v_mfma_f32_16x16x32_bf16 v[80:83], a[4:7], a[16:19], v[80:83]\n"
    "ds_read_b64_tr_b16 a[48:49], v52 offset:4096\n"
    "v_accvgpr_write_b32 a147, v63\n"
    "v_mfma_f32_16x16x32_bf16 v[68:71], a[0:3], a[20:23], v[68:71]\n"
    "ds_read_b64_tr_b16 a[50:51], v52 offset:4608\n"
    "v_cvt_pk_bf16_f32 v58, v180, v181\n"
    "v_mfma_f32_16x16x32_bf16 v[84:87], a[4:7], a[20:23], v[84:87]\n"
    "ds_read_b128 a[36:39], v45 offset:51200\n"
    "s_add_u32 m0, s36, 65536\n"
    "s_bfe_u32 s38, s58, 0x4001c\n"
    "s_lshl_b32 s38, s38, 18\n"
    "buffer_load_dwordx4 v16, s[24:27], s38 offen lds\n"
    "v_accvgpr_write_b32 a148, v56\n"
    "v_mfma_f32_16x16x32_bf16 v[96:99], a[8:11], a[16:19], v[96:99]\n"
    "ds_read_b64_tr_b16 a[52:53], v53 offset:4096\n"
    "v_cvt_pk_bf16_f32 v59, v182, v183\n"
    "v_mfma_f32_16x16x32_bf16 v[100:103], a[8:11], a[20:23], v[100:103]\n"
    "ds_read_b64_tr_b16 a[54:55], v53 offset:4608\n"
    "v_accvgpr_write_b32 a149, v57\n"
    "v_mfma_f32_16x16x32_bf16 v[72:75], a[0:3], a[24:27], v[72:75]\n"
    "ds_read_b128 a[40:43], v45 offset:53248\n"
    "v_cvt_pk_bf16_f32 v60, v168, v169\n"
    "v_mfma_f32_16x16x32_bf16 v[88:91], a[4:7], a[24:27], v[88:91]\n"
    "ds_read_b64_tr_b16 a[56:57], v54 offset:4096\n"
    "buffer_load_dwordx4 v17, s[24:27], s38 offen offset:1024 lds\n"
    "v_accvgpr_write_b32 a150, v58\n"
    "v_mfma_f32_16x16x32_bf16 v[104:107], a[8:11], a[24:27], v[104:107]\n"
    "ds_read_b64_tr_b16 a[58:59], v54 offset:4608\n"
    "v_cvt_pk_bf16_f32 v61, v170, v171\n"
    "s_waitcnt lgkmcnt(11)\n"
    "v_mfma_f32_16x16x32_bf16 v[112:115], a[12:15], a[16:19], v[112:115]\n"
    "ds_read_b128 a[44:47], v45 offset:55296\n"
    "v_accvgpr_write_b32 a151, v59\n"
    "v_mfma_f32_16x16x32_bf16 v[116:119], a[12:15], a[20:23], v[116:119]\n"
    "ds_read_b64_tr_b16 a[60:61], v55 offset:4096\n"
    "v_cvt_pk_bf16_f32 v62, v184, v185\n"
    "v_mfma_f32_16x16x32_bf16 v[120:123], a[12:15], a[24:27], v[120:123]\n"
    "ds_read_b64_tr_b16 a[62:63], v55 offset:4608\n"
    "buffer_load_dwordx4 v18, s[24:27], s38 offen offset:2048 lds\n"
    "v_accvgpr_write_b32 a152, v60\n"
    "s_waitcnt lgkmcnt(12)\n"
    "v_mfma_f32_16x16x32_bf16 v[76:79], a[0:3], a[28:31], v[76:79]\n"
    "v_cvt_pk_bf16_f32 v63, v186, v187\n"
    "v_mfma_f32_16x16x32_bf16 v[92:95], a[4:7], a[28:31], v[92:95]\n"
    "v_accvgpr_write_b32 a153, v61\n"
    "v_mfma_f32_16x16x32_bf16 v[108:111], a[8:11], a[28:31], v[108:111]\n"
    "v_cvt_pk_bf16_f32 v56, v140, v141\n"
    "v_mfma_f32_16x16x32_bf16 v[124:127], a[12:15], a[28:31], v[124:127]\n"
    "buffer_load_dwordx4 v19, s[24:27], s38 offen offset:3072 lds\n"
    "v_accvgpr_write_b32 a154, v62\n"
    "s_waitcnt vmcnt(17)\n"
    "s_barrier\n"
    "s_waitcnt lgkmcnt(3)\n"
    "v_mfma_f32_16x16x32_bf16 v[64:67], a[32:35], a[48:51], v[64:67]\n"
    "ds_read_b128 a[0:3], v46 offset:16384\n"
    "v_cvt_pk_bf16_f32 v57, v142, v143\n"
    "v_mfma_f32_16x16x32_bf16 v[80:83], a[36:39], a[48:51], v[80:83]\n"
    "ds_read_b64_tr_b16 a[16:17], v6 offset:0\n"
    "v_accvgpr_write_b32 a155, v63\n"
    "v_mfma_f32_16x16x32_bf16 v[68:71], a[32:35], a[52:55], v[68:71]\n"
    "ds_read_b64_tr_b16 a[18:19], v6 offset:512\n"
    "v_cvt_pk_bf16_f32 v58, v156, v157\n"
    "v_mfma_f32_16x16x32_bf16 v[84:87], a[36:39], a[52:55], v[84:87]\n"
    "ds_read_b128 a[4:7], v46 offset:18432\n"
    "s_add_u32 m0, s36, 81920\n"
    "s_bfe_u32 s38, s58, 0x4001c\n"
    "s_lshl_b32 s38, s38, 13\n"
    "s_or_b32 s38, s38, 128\n"
    "buffer_load_dwordx4 v12, s[20:23], s38 offen lds\n"
    "v_accvgpr_write_b32 a140, v56\n"
    "v_mfma_f32_16x16x32_bf16 v[96:99], a[40:43], a[48:51], v[96:99]\n"
    "ds_read_b64_tr_b16 a[20:21], v7 offset:0\n"
    "v_cvt_pk_bf16_f32 v59, v158, v159\n"
    "v_mfma_f32_16x16x32_bf16 v[100:103], a[40:43], a[52:55], v[100:103]\n"
    "ds_read_b64_tr_b16 a[22:23], v7 offset:512\n"
    "v_accvgpr_write_b32 a141, v57\n"
    "v_mfma_f32_16x16x32_bf16 v[72:75], a[32:35], a[56:59], v[72:75]\n"
    "ds_read_b128 a[8:11], v46 offset:20480\n"
    "v_cvt_pk_bf16_f32 v60, v172, v173\n"
    "v_mfma_f32_16x16x32_bf16 v[88:91], a[36:39], a[56:59], v[88:91]\n"
    "ds_read_b64_tr_b16 a[24:25], v8 offset:0\n"
    "buffer_load_dwordx4 v13, s[20:23], s38 offen offset:1024 lds\n"
    "v_accvgpr_write_b32 a142, v58\n"
    "v_mfma_f32_16x16x32_bf16 v[104:107], a[40:43], a[56:59], v[104:107]\n"
    "ds_read_b64_tr_b16 a[26:27], v8 offset:512\n"
    "v_cvt_pk_bf16_f32 v61, v174, v175\n"
    "s_waitcnt lgkmcnt(11)\n"
    "v_mfma_f32_16x16x32_bf16 v[112:115], a[44:47], a[48:51], v[112:115]\n"
    "ds_read_b128 a[12:15], v46 offset:22528\n"
    "v_accvgpr_write_b32 a143, v59\n"
    "v_mfma_f32_16x16x32_bf16 v[116:119], a[44:47], a[52:55], v[116:119]\n"
    "ds_read_b64_tr_b16 a[28:29], v9 offset:0\n"
    "v_cvt_pk_bf16_f32 v62, v188, v189\n"
    "v_mfma_f32_16x16x32_bf16 v[120:123], a[44:47], a[56:59], v[120:123]\n"
    "ds_read_b64_tr_b16 a[30:31], v9 offset:512\n"
    "buffer_load_dwordx4 v14, s[20:23], s38 offen offset:2048 lds\n"
    "v_accvgpr_write_b32 a156, v60\n"
    "s_waitcnt lgkmcnt(12)\n"
    "v_mfma_f32_16x16x32_bf16 v[76:79], a[32:35], a[60:63], v[76:79]\n"
    "v_cvt_pk_bf16_f32 v63, v190, v191\n"
    "v_mfma_f32_16x16x32_bf16 v[92:95], a[36:39], a[60:63], v[92:95]\n"
    "v_accvgpr_write_b32 a157, v61\n"
    "v_mfma_f32_16x16x32_bf16 v[108:111], a[40:43], a[60:63], v[108:111]\n"
    "v_accvgpr_write_b32 a158, v62\n"
    "v_mfma_f32_16x16x32_bf16 v[124:127], a[44:47], a[60:63], v[124:127]\n"
    "buffer_load_dwordx4 v15, s[20:23], s38 offen offset:3072 lds\n"
    "v_accvgpr_write_b32 a159, v63\n"
    "; ================ period 28 ================\n"
    "s_waitcnt lgkmcnt(3)\n"
    "v_mfma_f32_16x16x32_bf16 v[128:131], a[0:3], a[16:19], 0\n"
    "ds_read_b128 a[32:35], v47 offset:16384\n"
    "v_cvt_pk_bf16_f32 v56, v64, v65\n"
    "v_mfma_f32_16x16x32_bf16 v[144:147], a[4:7], a[16:19], 0\n"
    "ds_read_b64_tr_b16 a[48:49], v6 offset:4096\n"
    "v_cvt_pk_bf16_f32 v57, v66, v67\n"
    "v_mfma_f32_16x16x32_bf16 v[132:135], a[0:3], a[20:23], 0\n"
    "ds_read_b64_tr_b16 a[50:51], v6 offset:4608\n"
    "v_cvt_pk_bf16_f32 v58, v80, v81\n"
    "v_mfma_f32_16x16x32_bf16 v[148:151], a[4:7], a[20:23], 0\n"
    "ds_read_b128 a[36:39], v47 offset:18432\n"
    "s_add_u32 m0, s36, 98304\n"
    "s_bfe_u32 s38, s58, 0x4001c\n"
    "s_lshl_b32 s38, s38, 18\n"
    "s_or_b32 s38, s38, 131072\n"
    "buffer_load_dwordx4 v16, s[24:27], s38 offen lds\n"
    "v_accvgpr_write_b32 a160, v56\n"
    "v_mfma_f32_16x16x32_bf16 v[160:163], a[8:11], a[16:19], 0\n"
    "ds_read_b64_tr_b16 a[52:53], v7 offset:4096\n"
    "v_cvt_pk_bf16_f32 v59, v82, v83\n"
    "v_mfma_f32_16x16x32_bf16 v[164:167], a[8:11], a[20:23], 0\n"
    "ds_read_b64_tr_b16 a[54:55], v7 offset:4608\n"
    "v_accvgpr_write_b32 a161, v57\n"
    "v_mfma_f32_16x16x32_bf16 v[136:139], a[0:3], a[24:27], 0\n"
    "ds_read_b128 a[40:43], v47 offset:20480\n"
    "v_cvt_pk_bf16_f32 v60, v68, v69\n"
    "v_mfma_f32_16x16x32_bf16 v[152:155], a[4:7], a[24:27], 0\n"
    "ds_read_b64_tr_b16 a[56:57], v8 offset:4096\n"
    "buffer_load_dwordx4 v17, s[24:27], s38 offen offset:1024 lds\n"
    "v_accvgpr_write_b32 a162, v58\n"
    "v_mfma_f32_16x16x32_bf16 v[168:171], a[8:11], a[24:27], 0\n"
    "ds_read_b64_tr_b16 a[58:59], v8 offset:4608\n"
    "v_cvt_pk_bf16_f32 v61, v70, v71\n"
    "s_waitcnt lgkmcnt(11)\n"
    "v_mfma_f32_16x16x32_bf16 v[176:179], a[12:15], a[16:19], 0\n"
    "ds_read_b128 a[44:47], v47 offset:22528\n"
    "v_accvgpr_write_b32 a163, v59\n"
    "v_mfma_f32_16x16x32_bf16 v[180:183], a[12:15], a[20:23], 0\n"
    "ds_read_b64_tr_b16 a[60:61], v9 offset:4096\n"
    "v_cvt_pk_bf16_f32 v62, v84, v85\n"
    "v_mfma_f32_16x16x32_bf16 v[184:187], a[12:15], a[24:27], 0\n"
    "ds_read_b64_tr_b16 a[62:63], v9 offset:4608\n"
    "buffer_load_dwordx4 v18, s[24:27], s38 offen offset:2048 lds\n"
    "v_accvgpr_write_b32 a164, v60\n"
    "s_waitcnt lgkmcnt(12)\n"
    "v_mfma_f32_16x16x32_bf16 v[140:143], a[0:3], a[28:31], 0\n"
    "v_cvt_pk_bf16_f32 v63, v86, v87\n"
    "v_mfma_f32_16x16x32_bf16 v[156:159], a[4:7], a[28:31], 0\n"
    "v_accvgpr_write_b32 a165, v61\n"
    "v_mfma_f32_16x16x32_bf16 v[172:175], a[8:11], a[28:31], 0\n"
    "v_cvt_pk_bf16_f32 v56, v72, v73\n"
    "v_mfma_f32_16x16x32_bf16 v[188:191], a[12:15], a[28:31], 0\n"
    "buffer_load_dwordx4 v19, s[24:27], s38 offen offset:3072 lds\n"
    "v_accvgpr_write_b32 a166, v62\n"
    "s_waitcnt vmcnt(16)\n"
    "s_barrier\n"
    "v_cmp_ne_u64_e64 s[50:51], v[36:37], v[32:33]\n"
    "s_nop 4\n"
    "s_cmp_eq_u64 s[50:51], 0\n"
    "s_cbranch_scc1 Lp_ok_%=\n"
    "s_mov_b32 s44, 0x40000\n"
    "Lp_spin_%=:\n"
    "s_sleep 2\n"
    "buffer_load_dwordx2 v[36:37], v22, s[28:31], 0 offen sc1\n"
    "s_waitcnt vmcnt(0)\n"
    "v_cmp_ne_u64_e64 s[50:51], v[36:37], v[32:33]\n"
    "s_nop 4\n"
    "s_cmp_eq_u64 s[50:51], 0\n"
    "s_cbranch_scc1 Lp_ok_%=\n"
    "s_sub_u32 s44, s44, 1\n"
    "s_cmp_lg_u32 s44, 0\n"
    "s_cbranch_scc1 Lp_spin_%=\n"
    "Lp_ok_%=:\n"
    "s_mov_b64 exec, s[40:41]\n"
    "buffer_store_dwordx2 v[32:33], v29, s[28:31], 0 offen sc1\n"
    "s_mov_b64 exec, -1\n"
    "s_waitcnt lgkmcnt(3)\n"
    "v_mfma_f32_16x16x32_bf16 v[128:131], a[32:35], a[48:51], v[128:131]\n"
    "ds_read_b128 a[0:3], v4 offset:16384\n"
    "v_cvt_pk_bf16_f32 v57, v74, v75\n"
    "v_mfma_f32_16x16x32_bf16 v[144:147], a[36:39], a[48:51], v[144:147]\n"
    "ds_read_b64_tr_b16 a[16:17], v6 offset:32768\n"
    "v_accvgpr_write_b32 a167, v63\n"
    "v_mfma_f32_16x16x32_bf16 v[132:135], a[32:35], a[52:55], v[132:135]\n"
    "ds_read_b64_tr_b16 a[18:19], v6 offset:33280\n"
    "v_cvt_pk_bf16_f32 v58, v88, v89\n"
    "v_mfma_f32_16x16x32_bf16 v[148:151], a[36:39], a[52:55], v[148:151]\n"
    "ds_read_b128 a[4:7], v4 offset:18432\n"
    "v_accvgpr_write_b32 a168, v56\n"
    "v_mfma_f32_16x16x32_bf16 v[160:163], a[40:43], a[48:51], v[160:163]\n"
    "ds_read_b64_tr_b16 a[20:21], v7 offset:32768\n"
    "v_cvt_pk_bf16_f32 v59, v90, v91\n"
    "v_mfma_f32_16x16x32_bf16 v[164:167], a[40:43], a[52:55], v[164:167]\n"
    "ds_read_b64_tr_b16 a[22:23], v7 offset:33280\n"
    "v_accvgpr_write_b32 a169, v57\n"
    "v_mfma_f32_16x16x32_bf16 v[136:139], a[32:35], a[56:59], v[136:139]\n"
    "ds_read_b128 a[8:11], v4 offset:20480\n"
    "v_cvt_pk_bf16_f32 v60, v96, v97\n"
    "v_mfma_f32_16x16x32_bf16 v[152:155], a[36:39], a[56:59], v[152:155]\n"
    "ds_read_b64_tr_b16 a[24:25], v8 offset:32768\n"
    "v_accvgpr_write_b32 a170, v58\n"
    "v_mfma_f32_16x16x32_bf16 v[168:171], a[40:43], a[56:59], v[168:171]\n"
    "ds_read_b64_tr_b16 a[26:27], v8 offset:33280\n"
    "v_cvt_pk_bf16_f32 v61, v98, v99\n"
    "s_waitcnt lgkmcnt(11)\n"
    "v_mfma_f32_16x16x32_bf16 v[176:179], a[44:47], a[48:51], v[176:179]\n"
    "ds_read_b128 a[12:15], v4 offset:22528\n"
    "v_accvgpr_write_b32 a171, v59\n"
    "v_mfma_f32_16x16x32_bf16 v[180:183], a[44:47], a[52:55], v[180:183]\n"
    "ds_read_b64_tr_b16 a[28:29], v9 offset:32768\n"
    "v_cvt_pk_bf16_f32 v62, v112, v113\n"
    "v_mfma_f32_16x16x32_bf16 v[184:187], a[44:47], a[56:59], v[184:187]\n"
    "ds_read_b64_tr_b16 a[30:31], v9 offset:33280\n"
    "v_accvgpr_write_b32 a176, v60\n"
    "s_waitcnt lgkmcnt(12)\n"
    "v_mfma_f32_16x16x32_bf16 v[140:143], a[32:35], a[60:63], v[140:143]\n"
    "v_cvt_pk_bf16_f32 v63, v114, v115\n"
    "v_mfma_f32_16x16x32_bf16 v[156:159], a[36:39], a[60:63], v[156:159]\n"
    "v_accvgpr_write_b32 a177, v61\n"
    "v_mfma_f32_16x16x32_bf16 v[172:175], a[40:43], a[60:63], v[172:175]\n"
    "v_cvt_pk_bf16_f32 v56, v100, v101\n"
    "v_mfma_f32_16x16x32_bf16 v[188:191], a[44:47], a[60:63], v[188:191]\n"
    "v_accvgpr_write_b32 a178, v62\n"
    "; ================ period 29 ================\n"
    "s_waitcnt lgkmcnt(3)\n"
    "v_mfma_f32_16x16x32_bf16 v[128:131], a[0:3], a[16:19], v[128:131]\n"
    "ds_read_b128 a[32:35], v5 offset:16384\n"
    "v_cvt_pk_bf16_f32 v57, v102, v103\n"
    "v_mfma_f32_16x16x32_bf16 v[144:147], a[4:7], a[16:19], v[144:147]\n"
    "ds_read_b64_tr_b16 a[48:49], v6 offset:36864\n"
    "v_accvgpr_write_b32 a179, v63\n"
    "v_mfma_f32_16x16x32_bf16 v[132:135], a[0:3], a[20:23], v[132:135]\n"
    "ds_read_b64_tr_b16 a[50:51], v6 offset:37376\n"
    "v_cvt_pk_bf16_f32 v58, v116, v117\n"
    "v_mfma_f32_16x16x32_bf16 v[148:151], a[4:7], a[20:23], v[148:151]\n"
    "ds_read_b128 a[36:39], v5 offset:18432\n"
    "v_accvgpr_write_b32 a180, v56\n"
    "v_mfma_f32_16x16x32_bf16 v[160:163], a[8:11], a[16:19], v[160:163]\n"
    "ds_read_b64_tr_b16 a[52:53], v7 offset:36864\n"
    "v_cvt_pk_bf16_f32 v59, v118, v119\n"
    "v_mfma_f32_16x16x32_bf16 v[164:167], a[8:11], a[20:23], v[164:167]\n"
    "ds_read_b64_tr_b16 a[54:55], v7 offset:37376\n"
    "v_accvgpr_write_b32 a181, v57\n"
    "v_mfma_f32_16x16x32_bf16 v[136:139], a[0:3], a[24:27], v[136:139]\n"
    "ds_read_b128 a[40:43], v5 offset:20480\n"
    "v_cvt_pk_bf16_f32 v60, v104, v105\n"
    "v_mfma_f32_16x16x32_bf16 v[152:155], a[4:7], a[24:27], v[152:155]\n"
    "ds_read_b64_tr_b16 a[56:57], v8 offset:36864\n"
    "v_accvgpr_write_b32 a182, v58\n"
    "v_mfma_f32_16x16x32_bf16 v[168:171], a[8:11], a[24:27], v[168:171]\n"
    "ds_read_b64_tr_b16 a[58:59], v8 offset:37376\n"
    "v_cvt_pk_bf16_f32 v61, v106, v107\n"
    "s_waitcnt lgkmcnt(11)\n"
    "v_mfma_f32_16x16x32_bf16 v[176:179], a[12:15], a[16:19], v[176:179]\n"
    "ds_read_b128 a[44:47], v5 offset:22528\n"
    "v_accvgpr_write_b32 a183, v59\n"
    "v_mfma_f32_16x16x32_bf16 v[180:183], a[12:15], a[20:23], v[180:183]\n"
    "ds_read_b64_tr_b16 a[60:61], v9 offset:36864\n"
    "v_cvt_pk_bf16_f32 v62, v120, v121\n"
    "v_mfma_f32_16x16x32_bf16 v[184:187], a[12:15], a[24:27], v[184:187]\n"
    "ds_read_b64_tr_b16 a[62:63], v9 offset:37376\n"
    "v_accvgpr_write_b32 a184, v60\n"
    "s_waitcnt lgkmcnt(12)\n"
    "v_mfma_f32_16x16x32_bf16 v[140:143], a[0:3], a[28:31], v[140:143]\n"
    "v_cvt_pk_bf16_f32 v63, v122, v123\n"
    "v_mfma_f32_16x16x32_bf16 v[156:159], a[4:7], a[28:31], v[156:159]\n"
    "v_accvgpr_write_b32 a185, v61\n"
    "v_mfma_f32_16x16x32_bf16 v[172:175], a[8:11], a[28:31], v[172:175]\n"
    "v_cvt_pk_bf16_f32 v56, v76, v77\n"
    "v_mfma_f32_16x16x32_bf16 v[188:191], a[12:15], a[28:31], v[188:191]\n"
    "v_accvgpr_write_b32 a186, v62\n"
    "s_waitcnt vmcnt(8)\n"
    "s_barrier\n"
    "s_waitcnt lgkmcnt(3)\n"
    "v_mfma_f32_16x16x32_bf16 v[128:131], a[32:35], a[48:51], v[128:131]\n"
    "ds_read_b128 a[0:3], v4 offset:49152\n"
    "v_cvt_pk_bf16_f32 v57, v78, v79\n"
    "v_mfma_f32_16x16x32_bf16 v[144:147], a[36:39], a[48:51], v[144:147]\n"
    "ds_read_b64_tr_b16 a[16:17], v48 offset:0\n"
    "v_accvgpr_write_b32 a187, v63\n"
    "v_mfma_f32_16x16x32_bf16 v[132:135], a[32:35], a[52:55], v[132:135]\n"
    "ds_read_b64_tr_b16 a[18:19], v48 offset:512\n"
    "v_cvt_pk_bf16_f32 v58, v92, v93\n"
    "v_mfma_f32_16x16x32_bf16 v[148:151], a[36:39], a[52:55], v[148:151]\n"
    "ds_read_b128 a[4:7], v4 offset:51200\n"
    "s_add_u32 m0, s37, 131072\n"
    "s_bfe_u32 s38, s58, 0x40000\n"
    "s_lshl_b32 s38, s38, 11\n"
    "buffer_load_dwordx4 v20, s[28:31], s38 offen sc1 lds\n"
    "v_accvgpr_write_b32 a172, v56\n"
    "v_mfma_f32_16x16x32_bf16 v[160:163], a[40:43], a[48:51], v[160:163]\n"
    "ds_read_b64_tr_b16 a[20:21], v49 offset:0\n"
    "v_cvt_pk_bf16_f32 v59, v94, v95\n"
    "v_mfma_f32_16x16x32_bf16 v[164:167], a[40:43], a[52:55], v[164:167]\n"
    "ds_read_b64_tr_b16 a[22:23], v49 offset:512\n"
    "v_accvgpr_write_b32 a173, v57\n"
    "v_mfma_f32_16x16x32_bf16 v[136:139], a[32:35], a[56:59], v[136:139]\n"
    "ds_read_b128 a[8:11], v4 offset:53248\n"
    "v_cvt_pk_bf16_f32 v60, v108, v109\n"
    "v_mfma_f32_16x16x32_bf16 v[152:155], a[36:39], a[56:59], v[152:155]\n"
    "ds_read_b64_tr_b16 a[24:25], v50 offset:0\n"
    "buffer_load_dwordx4 v20, s[28:31], s38 offen offset:1024 sc1 lds\n"
    "v_accvgpr_write_b32 a174, v58\n"
    "v_mfma_f32_16x16x32_bf16 v[168:171], a[40:43], a[56:59], v[168:171]\n"
    "ds_read_b64_tr_b16 a[26:27], v50 offset:512\n"
    "v_cvt_pk_bf16_f32 v61, v110, v111\n"
    "s_waitcnt lgkmcnt(11)\n"
    "v_mfma_f32_16x16x32_bf16 v[176:179], a[44:47], a[48:51], v[176:179]\n"
    "ds_read_b128 a[12:15], v4 offset:55296\n"
    "v_accvgpr_write_b32 a175, v59\n"
    "v_mfma_f32_16x16x32_bf16 v[180:183], a[44:47], a[52:55], v[180:183]\n"
    "ds_read_b64_tr_b16 a[28:29], v51 offset:0\n"
    "v_cvt_pk_bf16_f32 v62, v124, v125\n"
    "v_mfma_f32_16x16x32_bf16 v[184:187], a[44:47], a[56:59], v[184:187]\n"
    "ds_read_b64_tr_b16 a[30:31], v51 offset:512\n"
    "s_add_u32 m0, s37, 133120\n"
    "s_bfe_u32 s38, s58, 0x40004\n"
    "s_lshl_b32 s38, s38, 11\n"
    "buffer_load_dwordx4 v20, s[28:31], s38 offen sc1 lds\n"
    "v_accvgpr_write_b32 a188, v60\n"
    "s_waitcnt lgkmcnt(12)\n"
    "v_mfma_f32_16x16x32_bf16 v[140:143], a[32:35], a[60:63], v[140:143]\n"
    "v_cvt_pk_bf16_f32 v63, v126, v127\n"
    "v_mfma_f32_16x16x32_bf16 v[156:159], a[36:39], a[60:63], v[156:159]\n"
    "v_accvgpr_write_b32 a189, v61\n"
    "v_mfma_f32_16x16x32_bf16 v[172:175], a[40:43], a[60:63], v[172:175]\n"
    "v_accvgpr_write_b32 a190, v62\n"
    "v_mfma_f32_16x16x32_bf16 v[188:191], a[44:47], a[60:63], v[188:191]\n"
    "buffer_load_dwordx4 v20, s[28:31], s38 offen offset:1024 sc1 lds\n"
    "v_accvgpr_write_b32 a191, v63\n"
    "; ================ period 30 ================\n"
    "s_waitcnt lgkmcnt(3)\n"
    "v_mfma_f32_16x16x32_bf16 v[64:67], a[0:3], a[16:19], 0\n"
    "ds_read_b128 a[32:35], v5 offset:49152\n"
    "v_cvt_pk_bf16_f32 v56, v128, v129\n"
    "v_mfma_f32_16x16x32_bf16 v[80:83], a[4:7], a[16:19], 0\n"
    "ds_read_b64_tr_b16 a[48:49], v48 offset:4096\n"
    "v_cvt_pk_bf16_f32 v57, v130, v131\n"
    "v_mfma_f32_16x16x32_bf16 v[68:71], a[0:3], a[20:23], 0\n"
    "ds_read_b64_tr_b16 a[50:51], v48 offset:4608\n"
    "v_cvt_pk_bf16_f32 v58, v144, v145\n"
    "v_mfma_f32_16x16x32_bf16 v[84:87], a[4:7], a[20:23], 0\n"
    "ds_read_b128 a[36:39], v5 offset:51200\n"
    "s_add_u32 m0, s37, 135168\n"
    "s_bfe_u32 s38, s58, 0x40008\n"
    "s_lshl_b32 s38, s38, 11\n"
    "buffer_load_dwordx4 v20, s[28:31], s38 offen sc1 lds\n"
    "v_accvgpr_write_b32 a192, v56\n"
    "v_mfma_f32_16x16x32_bf16 v[96:99], a[8:11], a[16:19], 0\n"
    "ds_read_b64_tr_b16 a[52:53], v49 offset:4096\n"
    "v_cvt_pk_bf16_f32 v59, v146, v147\n"
    "v_mfma_f32_16x16x32_bf16 v[100:103], a[8:11], a[20:23], 0\n"
    "ds_read_b64_tr_b16 a[54:55], v49 offset:4608\n"
    "v_accvgpr_write_b32 a193, v57\n"
    "v_mfma_f32_16x16x32_bf16 v[72:75], a[0:3], a[24:27], 0\n"
    "ds_read_b128 a[40:43], v5 offset:53248\n"
    "v_cvt_pk_bf16_f32 v60, v132, v133\n"
    "v_mfma_f32_16x16x32_bf16 v[88:91], a[4:7], a[24:27], 0\n"
    "ds_read_b64_tr_b16 a[56:57], v50 offset:4096\n"
    "buffer_load_dwordx4 v20, s[28:31], s38 offen offset:1024 sc1 lds\n"
    "v_accvgpr_write_b32 a194, v58\n"
    "v_mfma_f32_16x16x32_bf16 v[104:107], a[8:11], a[24:27], 0\n"
    "ds_read_b64_tr_b16 a[58:59], v50 offset:4608\n"
    "v_cvt_pk_bf16_f32 v61, v134, v135\n"
    "s_waitcnt lgkmcnt(11)\n"
    "v_mfma_f32_16x16x32_bf16 v[112:115], a[12:15], a[16:19], 0\n"
    "ds_read_b128 a[44:47], v5 offset:55296\n"
    "v_accvgpr_write_b32 a195, v59\n"
    "v_mfma_f32_16x16x32_bf16 v[116:119], a[12:15], a[20:23], 0\n"
    "ds_read_b64_tr_b16 a[60:61], v51 offset:4096\n"
    "v_cvt_pk_bf16_f32 v62, v148, v149\n"
    "v_mfma_f32_16x16x32_bf16 v[120:123], a[12:15], a[24:27], 0\n"
    "ds_read_b64_tr_b16 a[62:63], v51 offset:4608\n"
    "s_add_u32 m0, s37, 137216\n"
    "s_bfe_u32 s38, s58, 0x4000c\n"
    "s_lshl_b32 s38, s38, 11\n"
    "buffer_load_dwordx4 v20, s[28:31], s38 offen sc1 lds\n"
    "v_accvgpr_write_b32 a196, v60\n"
    "s_waitcnt lgkmcnt(12)\n"
    "v_mfma_f32_16x16x32_bf16 v[76:79], a[0:3], a[28:31], 0\n"
    "v_cvt_pk_bf16_f32 v63, v150, v151\n"
    "v_mfma_f32_16x16x32_bf16 v[92:95], a[4:7], a[28:31], 0\n"
    "v_accvgpr_write_b32 a197, v61\n"
    "v_mfma_f32_16x16x32_bf16 v[108:111], a[8:11], a[28:31], 0\n"
    "v_cvt_pk_bf16_f32 v56, v136, v137\n"
    "v_mfma_f32_16x16x32_bf16 v[124:127], a[12:15], a[28:31], 0\n"
    "buffer_load_dwordx4 v20, s[28:31], s38 offen offset:1024 sc1 lds\n"
    "v_accvgpr_write_b32 a198, v62\n"
    "s_waitcnt vmcnt(8)\n"
    "s_barrier\n"
    "s_waitcnt lgkmcnt(3)\n"
    "v_mfma_f32_16x16x32_bf16 v[64:67], a[32:35], a[48:51], v[64:67]\n"
    "ds_read_b128 a[0:3], v44 offset:16384\n"
    "v_cvt_pk_bf16_f32 v57, v138, v139\n"
    "v_mfma_f32_16x16x32_bf16 v[80:83], a[36:39], a[48:51], v[80:83]\n"
    "ds_read_b64_tr_b16 a[16:17], v48 offset:32768\n"
    "v_accvgpr_write_b32 a199, v63\n"
    "v_mfma_f32_16x16x32_bf16 v[68:71], a[32:35], a[52:55], v[68:71]\n"
    "ds_read_b64_tr_b16 a[18:19], v48 offset:33280\n"
    "v_cvt_pk_bf16_f32 v58, v152, v153\n"
    "v_mfma_f32_16x16x32_bf16 v[84:87], a[36:39], a[52:55], v[84:87]\n"
    "ds_read_b128 a[4:7], v44 offset:18432\n"
    "s_add_u32 m0, s37, 0\n"
    "s_bfe_u32 s38, s58, 0x40010\n"
    "s_lshl_b32 s38, s38, 11\n"
    "buffer_load_dwordx4 v20, s[28:31], s38 offen sc1 lds\n"
    "v_accvgpr_write_b32 a200, v56\n"
    "v_mfma_f32_16x16x32_bf16 v[96:99], a[40:43], a[48:51], v[96:99]\n"
    "ds_read_b64_tr_b16 a[20:21], v49 offset:32768\n"
    "v_cvt_pk_bf16_f32 v59, v154, v155\n"
    "v_mfma_f32_16x16x32_bf16 v[100:103], a[40:43], a[52:55], v[100:103]\n"
    "ds_read_b64_tr_b16 a[22:23], v49 offset:33280\n"
    "v_accvgpr_write_b32 a201, v57\n"
    "v_mfma_f32_16x16x32_bf16 v[72:75], a[32:35], a[56:59], v[72:75]\n"
    "ds_read_b128 a[8:11], v44 offset:20480\n"
    "v_cvt_pk_bf16_f32 v60, v160, v161\n"
    "v_mfma_f32_16x16x32_bf16 v[88:91], a[36:39], a[56:59], v[88:91]\n"
    "ds_read_b64_tr_b16 a[24:25], v50 offset:32768\n"
    "buffer_load_dwordx4 v20, s[28:31], s38 offen offset:1024 sc1 lds\n"
    "v_accvgpr_write_b32 a202, v58\n"
    "v_mfma_f32_16x16x32_bf16 v[104:107], a[40:43], a[56:59], v[104:107]\n"
    "ds_read_b64_tr_b16 a[26:27], v50 offset:33280\n"
    "v_cvt_pk_bf16_f32 v61, v162, v163\n"
    "s_waitcnt lgkmcnt(11)\n"
    "v_mfma_f32_16x16x32_bf16 v[112:115], a[44:47], a[48:51], v[112:115]\n"
    "ds_read_b128 a[12:15], v44 offset:22528\n"
    "v_accvgpr_write_b32 a203, v59\n"
    "v_mfma_f32_16x16x32_bf16 v[116:119], a[44:47], a[52:55], v[116:119]\n"
    "ds_read_b64_tr_b16 a[28:29], v51 offset:32768\n"
    "v_cvt_pk_bf16_f32 v62, v176, v177\n"
    "v_mfma_f32_16x16x32_bf16 v[120:123], a[44:47], a[56:59], v[120:123]\n"
    "ds_read_b64_tr_b16 a[30:31], v51 offset:33280\n"
    "s_add_u32 m0, s37, 2048\n"
    "s_bfe_u32 s38, s58, 0x40014\n"
    "s_lshl_b32 s38, s38, 11\n"
    "buffer_load_dwordx4 v20, s[28:31], s38 offen sc1 lds\n"
    "v_accvgpr_write_b32 a208, v60\n"
    "s_waitcnt lgkmcnt(12)\n"
    "v_mfma_f32_16x16x32_bf16 v[76:79], a[32:35], a[60:63], v[76:79]\n"
    "v_cvt_pk_bf16_f32 v63, v178, v179\n"
    "v_mfma_f32_16x16x32_bf16 v[92:95], a[36:39], a[60:63], v[92:95]\n"
    "v_accvgpr_write_b32 a209, v61\n"
    "v_mfma_f32_16x16x32_bf16 v[108:111], a[40:43], a[60:63], v[108:111]\n"
    "v_cvt_pk_bf16_f32 v56, v164, v165\n"
    "v_mfma_f32_16x16x32_bf16 v[124:127], a[44:47], a[60:63], v[124:127]\n"
    "buffer_load_dwordx4 v20, s[28:31], s38 offen offset:1024 sc1 lds\n"
    "v_accvgpr_write_b32 a210, v62\n"
    "; ================ period 31 ================\n"
    "s_waitcnt lgkmcnt(3)\n"
    "v_mfma_f32_16x16x32_bf16 v[64:67], a[0:3], a[16:19], v[64:67]\n"
    "ds_read_b128 a[32:35], v45 offset:16384\n"
    "v_cvt_pk_bf16_f32 v57, v166, v167\n"
    "v_mfma_f32_16x16x32_bf16 v[80:83], a[4:7], a[16:19], v[80:83]\n"
    "ds_read_b64_tr_b16 a[48:49], v48 offset:36864\n"
    "v_accvgpr_write_b32 a211, v63\n"
    "v_mfma_f32_16x16x32_bf16 v[68:71], a[0:3], a[20:23], v[68:71]\n"
    "ds_read_b64_tr_b16 a[50:51], v48 offset:37376\n"
    "v_cvt_pk_bf16_f32 v58, v180, v181\n"
    "v_mfma_f32_16x16x32_bf16 v[84:87], a[4:7], a[20:23], v[84:87]\n"
    "ds_read_b128 a[36:39], v45 offset:18432\n"
    "s_add_u32 m0, s37, 4096\n"
    "s_bfe_u32 s38, s58, 0x40018\n"
    "s_lshl_b32 s38, s38, 11\n"
    "buffer_load_dwordx4 v20, s[28:31], s38 offen sc1 lds\n"
    "v_accvgpr_write_b32 a212, v56\n"
    "v_mfma_f32_16x16x32_bf16 v[96:99], a[8:11], a[16:19], v[96:99]\n"
    "ds_read_b64_tr_b16 a[52:53], v49 offset:36864\n"
    "v_cvt_pk_bf16_f32 v59, v182, v183\n"
    "v_mfma_f32_16x16x32_bf16 v[100:103], a[8:11], a[20:23], v[100:103]\n"
    "ds_read_b64_tr_b16 a[54:55], v49 offset:37376\n"
    "v_accvgpr_write_b32 a213, v57\n"
    "v_mfma_f32_16x16x32_bf16 v[72:75], a[0:3], a[24:27], v[72:75]\n"
    "ds_read_b128 a[40:43], v45 offset:20480\n"
    "v_cvt_pk_bf16_f32 v60, v168, v169\n"
    "v_mfma_f32_16x16x32_bf16 v[88:91], a[4:7], a[24:27], v[88:91]\n"
    "ds_read_b64_tr_b16 a[56:57], v50 offset:36864\n"
    "buffer_load_dwordx4 v20, s[28:31], s38 offen offset:1024 sc1 lds\n"
    "v_accvgpr_write_b32 a214, v58\n"
    "v_mfma_f32_16x16x32_bf16 v[104:107], a[8:11], a[24:27], v[104:107]\n"
    "ds_read_b64_tr_b16 a[58:59], v50 offset:37376\n"
    "v_cvt_pk_bf16_f32 v61, v170, v171\n"
    "s_waitcnt lgkmcnt(11)\n"
    "v_mfma_f32_16x16x32_bf16 v[112:115], a[12:15], a[16:19], v[112:115]\n"
    "ds_read_b128 a[44:47], v45 offset:22528\n"
    "v_accvgpr_write_b32 a215, v59\n"
    "v_mfma_f32_16x16x32_bf16 v[116:119], a[12:15], a[20:23], v[116:119]\n"
    "ds_read_b64_tr_b16 a[60:61], v51 offset:36864\n"
    "v_cvt_pk_bf16_f32 v62, v184, v185\n"
    "v_mfma_f32_16x16x32_bf16 v[120:123], a[12:15], a[24:27], v[120:123]\n"
    "ds_read_b64_tr_b16 a[62:63], v51 offset:37376\n"
    "s_add_u32 m0, s37, 6144\n"
    "s_bfe_u32 s38, s58, 0x4001c\n"
    "s_lshl_b32 s38, s38, 11\n"
    "buffer_load_dwordx4 v20, s[28:31], s38 offen sc1 lds\n"
    "v_accvgpr_write_b32 a216, v60\n"
    "s_waitcnt lgkmcnt(12)\n"
    "v_mfma_f32_16x16x32_bf16 v[76:79], a[0:3], a[28:31], v[76:79]\n"
    "v_cvt_pk_bf16_f32 v63, v186, v187\n"
    "v_mfma_f32_16x16x32_bf16 v[92:95], a[4:7], a[28:31], v[92:95]\n"
    "v_accvgpr_write_b32 a217, v61\n"
    "v_mfma_f32_16x16x32_bf16 v[108:111], a[8:11], a[28:31], v[108:111]\n"
    "v_cvt_pk_bf16_f32 v56, v140, v141\n"
    "v_mfma_f32_16x16x32_bf16 v[124:127], a[12:15], a[28:31], v[124:127]\n"
    "buffer_load_dwordx4 v20, s[28:31], s38 offen offset:1024 sc1 lds\n"
    "v_accvgpr_write_b32 a218, v62\n"
    "s_waitcnt vmcnt(8)\n"
    "s_barrier\n"
    "s_waitcnt lgkmcnt(3)\n"
    "v_mfma_f32_16x16x32_bf16 v[64:67], a[32:35], a[48:51], v[64:67]\n"
    "v_cvt_pk_bf16_f32 v57, v142, v143\n"
    "v_mfma_f32_16x16x32_bf16 v[80:83], a[36:39], a[48:51], v[80:83]\n"
    "v_accvgpr_write_b32 a219, v63\n"
    "v_mfma_f32_16x16x32_bf16 v[68:71], a[32:35], a[52:55], v[68:71]\n"
    "v_cvt_pk_bf16_f32 v58, v156, v157\n"
    "v_mfma_f32_16x16x32_bf16 v[84:87], a[36:39], a[52:55], v[84:87]\n"
    "v_accvgpr_write_b32 a204, v56\n"
    "v_mfma_f32_16x16x32_bf16 v[96:99], a[40:43], a[48:51], v[96:99]\n"
    "v_cvt_pk_bf16_f32 v59, v158, v159\n"
    "v_mfma_f32_16x16x32_bf16 v[100:103], a[40:43], a[52:55], v[100:103]\n"
    "v_accvgpr_write_b32 a205, v57\n"
    "v_mfma_f32_16x16x32_bf16 v[72:75], a[32:35], a[56:59], v[72:75]\n"
    "v_cvt_pk_bf16_f32 v60, v172, v173\n"
    "v_mfma_f32_16x16x32_bf16 v[88:91], a[36:39], a[56:59], v[88:91]\n"
    "v_accvgpr_write_b32 a206, v58\n"
    "v_mfma_f32_16x16x32_bf16 v[104:107], a[40:43], a[56:59], v[104:107]\n"
    "v_cvt_pk_bf16_f32 v61, v174, v175\n"
    "s_waitcnt lgkmcnt(2)\n"
    "v_mfma_f32_16x16x32_bf16 v[112:115], a[44:47], a[48:51], v[112:115]\n"
    "v_accvgpr_write_b32 a207, v59\n"
    "v_mfma_f32_16x16x32_bf16 v[116:119], a[44:47], a[52:55], v[116:119]\n"
    "v_cvt_pk_bf16_f32 v62, v188, v189\n"
    "v_mfma_f32_16x16x32_bf16 v[120:123], a[44:47], a[56:59], v[120:123]\n"
    "v_accvgpr_write_b32 a220, v60\n"
    "s_waitcnt lgkmcnt(0)\n"
    "v_mfma_f32_16x16x32_bf16 v[76:79], a[32:35], a[60:63], v[76:79]\n"
    "v_cvt_pk_bf16_f32 v63, v190, v191\n"
    "v_mfma_f32_16x16x32_bf16 v[92:95], a[36:39], a[60:63], v[92:95]\n"
    "v_accvgpr_write_b32 a221, v61\n"
    "v_mfma_f32_16x16x32_bf16 v[108:111], a[40:43], a[60:63], v[108:111]\n"
    "v_accvgpr_write_b32 a222, v62\n"
    "v_mfma_f32_16x16x32_bf16 v[124:127], a[44:47], a[60:63], v[124:127]\n"
    "v_accvgpr_write_b32 a223, v63\n"
    "; ================ phase 3 ================\n"
    "ds_read_b128 a[0:3], v31 offset:0\n"
    "ds_read_b128 a[4:7], v31 offset:1024\n"
    "ds_read_b128 a[8:11], v31 offset:2048\n"
    "ds_read_b128 a[12:15], v31 offset:3072\n"
    "s_waitcnt lgkmcnt(3)\n"
    "v_mfma_f32_16x16x32_bf16 v[128:131], v[192:195], a[0:3], 0\n"
    "ds_read_b128 a[16:19], v31 offset:4096\n"
    "v_cvt_pk_bf16_f32 v64, v64, v65\n"
    "v_mfma_f32_16x16x32_bf16 v[136:139], v[196:199], a[0:3], 0\n"
    "v_cvt_pk_bf16_f32 v65, v66, v67\n"
    "s_waitcnt lgkmcnt(3)\n"
    "v_mfma_f32_16x16x32_bf16 v[132:135], v[192:195], a[4:7], 0\n"
    "ds_read_b128 a[20:23], v31 offset:5120\n"
    "v_cvt_pk_bf16_f32 v66, v80, v81\n"
    "v_mfma_f32_16x16x32_bf16 v[140:143], v[196:199], a[4:7], 0\n"
    "v_cvt_pk_bf16_f32 v67, v82, v83\n"
    "s_waitcnt lgkmcnt(3)\n"
    "v_mfma_f32_16x16x32_bf16 v[128:131], v[224:227], a[8:11], v[128:131]\n"
    "ds_read_b128 a[24:27], v31 offset:6144\n"
    "v_cvt_pk_bf16_f32 v68, v68, v69\n"
    "v_mfma_f32_16x16x32_bf16 v[136:139], v[228:231], a[8:11], v[136:139]\n"
    "v_cvt_pk_bf16_f32 v69, v70, v71\n"
    "s_waitcnt lgkmcnt(3)\n"
    "v_mfma_f32_16x16x32_bf16 v[132:135], v[224:227], a[12:15], v[132:135]\n"
    "ds_read_b128 a[28:31], v31 offset:7168\n"
    "v_cvt_pk_bf16_f32 v70, v84, v85\n"
    "v_mfma_f32_16x16x32_bf16 v[140:143], v[228:231], a[12:15], v[140:143]\n"
    "v_cvt_pk_bf16_f32 v71, v86, v87\n"
    "s_waitcnt lgkmcnt(3)\n"
    "v_mfma_f32_16x16x32_bf16 v[128:131], a[64:67], a[16:19], v[128:131]\n"
    "s_waitcnt vmcnt(0)\n"
    "s_barrier\n"
    "ds_read_b128 a[0:3], v42 offset:0\n"
    "v_cvt_pk_bf16_f32 v72, v72, v73\n"
    "v_mfma_f32_16x16x32_bf16 v[136:139], a[68:71], a[16:19], v[136:139]\n"
    "v_cvt_pk_bf16_f32 v73, v74, v75\n"
    "s_waitcnt lgkmcnt(3)\n"
    "v_mfma_f32_16x16x32_bf16 v[132:135], a[64:67], a[20:23], v[132:135]\n"
    "ds_read_b128 a[4:7], v42 offset:1024\n"
    "v_cvt_pk_bf16_f32 v74, v88, v89\n"
    "v_mfma_f32_16x16x32_bf16 v[140:143], a[68:71], a[20:23], v[140:143]\n"
    "v_cvt_pk_bf16_f32 v75, v90, v91\n"
    "s_waitcnt lgkmcnt(3)\n"
    "v_mfma_f32_16x16x32_bf16 v[128:131], a[96:99], a[24:27], v[128:131]\n"
    "ds_read_b128 a[8:11], v42 offset:2048\n"
    "v_cvt_pk_bf16_f32 v76, v76, v77\n"
    "v_mfma_f32_16x16x32_bf16 v[136:139], a[100:103], a[24:27], v[136:139]\n"
    "v_cvt_pk_bf16_f32 v77, v78, v79\n"
    "s_waitcnt lgkmcnt(3)\n"
    "v_mfma_f32_16x16x32_bf16 v[132:135], a[96:99], a[28:31], v[132:135]\n"
    "ds_read_b128 a[12:15], v42 offset:3072\n"
    "v_cvt_pk_bf16_f32 v78, v92, v93\n"
    "v_mfma_f32_16x16x32_bf16 v[140:143], a[100:103], a[28:31], v[140:143]\n"
    "v_cvt_pk_bf16_f32 v79, v94, v95\n"
    "s_waitcnt lgkmcnt(3)\n"
    "v_mfma_f32_16x16x32_bf16 v[128:131], a[128:131], a[0:3], v[128:131]\n"
    "ds_read_b128 a[16:19], v42 offset:4096\n"
    "v_cvt_pk_bf16_f32 v96, v96, v97\n"
    "v_mfma_f32_16x16x32_bf16 v[136:139], a[132:135], a[0:3], v[136:139]\n"
    "v_cvt_pk_bf16_f32 v97, v98, v99\n"
    "s_waitcnt lgkmcnt(3)\n"
    "v_mfma_f32_16x16x32_bf16 v[132:135], a[128:131], a[4:7], v[132:135]\n"
    "ds_read_b128 a[20:23], v42 offset:5120\n"
    "v_cvt_pk_bf16_f32 v98, v112, v113\n"
    "v_mfma_f32_16x16x32_bf16 v[140:143], a[132:135], a[4:7], v[140:143]\n"
    "v_cvt_pk_bf16_f32 v99, v114, v115\n"
    "s_waitcnt lgkmcnt(3)\n"
    "v_mfma_f32_16x16x32_bf16 v[128:131], a[160:163], a[8:11], v[128:131]\n"
    "ds_read_b128 a[24:27], v42 offset:6144\n"
    "v_cvt_pk_bf16_f32 v100, v100, v101\n"
    "v_mfma_f32_16x16x32_bf16 v[136:139], a[164:167], a[8:11], v[136:139]\n"
    "v_cvt_pk_bf16_f32 v101, v102, v103\n"
    "s_waitcnt lgkmcnt(3)\n"
    "v_mfma_f32_16x16x32_bf16 v[132:135], a[160:163], a[12:15], v[132:135]\n"
    "ds_read_b128 a[28:31], v42 offset:7168\n"
    "v_cvt_pk_bf16_f32 v102, v116, v117\n"
    "v_mfma_f32_16x16x32_bf16 v[140:143], a[164:167], a[12:15], v[140:143]\n"
    "v_cvt_pk_bf16_f32 v103, v118, v119\n"
    "s_waitcnt lgkmcnt(3)\n"
    "v_mfma_f32_16x16x32_bf16 v[128:131], a[192:195], a[16:19], v[128:131]\n"
    "ds_read_b128 a[0:3], v31 offset:0\n"
    "v_cvt_pk_bf16_f32 v104, v104, v105\n"
    "v_mfma_f32_16x16x32_bf16 v[136:139], a[196:199], a[16:19], v[136:139]\n"
    "v_cvt_pk_bf16_f32 v105, v106, v107\n"
    "s_waitcnt lgkmcnt(3)\n"
    "v_mfma_f32_16x16x32_bf16 v[132:135], a[192:195], a[20:23], v[132:135]\n"
    "ds_read_b128 a[4:7], v31 offset:1024\n"
    "v_cvt_pk_bf16_f32 v106, v120, v121\n"
    "v_mfma_f32_16x16x32_bf16 v[140:143], a[196:199], a[20:23], v[140:143]\n"
    "v_cvt_pk_bf16_f32 v107, v122, v123\n"
    "s_waitcnt lgkmcnt(3)\n"
    "v_mfma_f32_16x16x32_bf16 v[128:131], v[64:67], a[24:27], v[128:131]\n"
    "ds_read_b128 a[8:11], v31 offset:2048\n"
    "v_cvt_pk_bf16_f32 v108, v108, v109\n"
    "v_mfma_f32_16x16x32_bf16 v[136:139], v[68:71], a[24:27], v[136:139]\n"
    "v_cvt_pk_bf16_f32 v109, v110, v111\n"
    "s_waitcnt lgkmcnt(3)\n"
    "v_mfma_f32_16x16x32_bf16 v[132:135], v[64:67], a[28:31], v[132:135]\n"
    "ds_read_b128 a[12:15], v31 offset:3072\n"
    "v_cvt_pk_bf16_f32 v110, v124, v125\n"
    "v_mfma_f32_16x16x32_bf16 v[140:143], v[68:71], a[28:31], v[140:143]\n"
    "v_cvt_pk_bf16_f32 v111, v126, v127\n"
    "s_waitcnt lgkmcnt(3)\n"
    "v_mfma_f32_16x16x32_bf16 v[144:147], v[200:203], a[0:3], 0\n"
    "ds_read_b128 a[16:19], v31 offset:4096\n"
    "v_mfma_f32_16x16x32_bf16 v[152:155], v[204:207], a[0:3], 0\n"
    "s_waitcnt lgkmcnt(3)\n"
    "v_mfma_f32_16x16x32_bf16 v[148:151], v[200:203], a[4:7], 0\n"
    "ds_read_b128 a[20:23], v31 offset:5120\n"
    "v_mfma_f32_16x16x32_bf16 v[156:159], v[204:207], a[4:7], 0\n"
    "s_waitcnt lgkmcnt(3)\n"
    "v_mfma_f32_16x16x32_bf16 v[144:147], v[232:235], a[8:11], v[144:147]\n"
    "ds_read_b128 a[24:27], v31 offset:6144\n"
    "v_mfma_f32_16x16x32_bf16 v[152:155], v[236:239], a[8:11], v[152:155]\n"
    "s_waitcnt lgkmcnt(3)\n"
    "v_mfma_f32_16x16x32_bf16 v[148:151], v[232:235], a[12:15], v[148:151]\n"
    "ds_read_b128 a[28:31], v31 offset:7168\n"
    "v_cvt_pk_bf16_f32 v80, v128, v129\n"
    "v_mfma_f32_16x16x32_bf16 v[156:159], v[236:239], a[12:15], v[156:159]\n"
    "v_cvt_pk_bf16_f32 v81, v130, v131\n"
    "s_waitcnt lgkmcnt(3)\n"
    "v_mfma_f32_16x16x32_bf16 v[144:147], a[72:75], a[16:19], v[144:147]\n"
    "ds_read_b128 a[0:3], v42 offset:0\n"
    "v_cvt_pk_bf16_f32 v82, v132, v133\n"
    "v_mfma_f32_16x16x32_bf16 v[152:155], a[76:79], a[16:19], v[152:155]\n"
    "v_cvt_pk_bf16_f32 v83, v134, v135\n"
    "s_waitcnt lgkmcnt(3)\n"
    "v_mfma_f32_16x16x32_bf16 v[148:151], a[72:75], a[20:23], v[148:151]\n"
    "ds_read_b128 a[4:7], v42 offset:1024\n"
    "v_cvt_pk_bf16_f32 v84, v136, v137\n"
    "v_mfma_f32_16x16x32_bf16 v[156:159], a[76:79], a[20:23], v[156:159]\n"
    "v_cvt_pk_bf16_f32 v85, v138, v139\n"
    "s_waitcnt lgkmcnt(3)\n"
    "v_mfma_f32_16x16x32_bf16 v[144:147], a[104:107], a[24:27], v[144:147]\n"
    "ds_read_b128 a[8:11], v42 offset:2048\n"
    "v_cvt_pk_bf16_f32 v86, v140, v141\n"
    "v_mfma_f32_16x16x32_bf16 v[152:155], a[108:111], a[24:27], v[152:155]\n"
    "v_cvt_pk_bf16_f32 v87, v142, v143\n"
    "s_waitcnt lgkmcnt(3)\n"
    "v_mfma_f32_16x16x32_bf16 v[148:151], a[104:107], a[28:31], v[148:151]\n"
    "ds_read_b128 a[12:15], v42 offset:3072\n"
    "buffer_store_dwordx4 v[80:83], v24, s[28:31], 0 offen sc1\n"
    "v_mfma_f32_16x16x32_bf16 v[156:159], a[108:111], a[28:31], v[156:159]\n"
    "buffer_store_dwordx4 v[84:87], v24, s[28:31], 0 offen offset:1024 sc1\n"
    "s_waitcnt lgkmcnt(3)\n"
    "v_mfma_f32_16x16x32_bf16 v[144:147], a[136:139], a[0:3], v[144:147]\n"
    "ds_read_b128 a[16:19], v42 offset:4096\n"
    "v_mfma_f32_16x16x32_bf16 v[152:155], a[140:143], a[0:3], v[152:155]\n"
    "s_waitcnt lgkmcnt(3)\n"
    "v_mfma_f32_16x16x32_bf16 v[148:151], a[136:139], a[4:7], v[148:151]\n"
    "ds_read_b128 a[20:23], v42 offset:5120\n"
    "v_mfma_f32_16x16x32_bf16 v[156:159], a[140:143], a[4:7], v[156:159]\n"
    "s_waitcnt lgkmcnt(3)\n"
    "v_mfma_f32_16x16x32_bf16 v[144:147], a[168:171], a[8:11], v[144:147]\n"
    "ds_read_b128 a[24:27], v42 offset:6144\n"
    "v_mfma_f32_16x16x32_bf16 v[152:155], a[172:175], a[8:11], v[152:155]\n"
    "s_waitcnt lgkmcnt(3)\n"
    "v_mfma_f32_16x16x32_bf16 v[148:151], a[168:171], a[12:15], v[148:151]\n"
    "ds_read_b128 a[28:31], v42 offset:7168\n"
    "v_mfma_f32_16x16x32_bf16 v[156:159], a[172:175], a[12:15], v[156:159]\n"
    "s_waitcnt lgkmcnt(3)\n"
    "v_mfma_f32_16x16x32_bf16 v[144:147], a[200:203], a[16:19], v[144:147]\n"
    "ds_read_b128 a[0:3], v31 offset:8192\n"
    "v_mfma_f32_16x16x32_bf16 v[152:155], a[204:207], a[16:19], v[152:155]\n"
    "s_waitcnt lgkmcnt(3)\n"
    "v_mfma_f32_16x16x32_bf16 v[148:151], a[200:203], a[20:23], v[148:151]\n"
    "ds_read_b128 a[4:7], v31 offset:9216\n"
    "v_mfma_f32_16x16x32_bf16 v[156:159], a[204:207], a[20:23], v[156:159]\n"
    "s_waitcnt lgkmcnt(3)\n"
    "v_mfma_f32_16x16x32_bf16 v[144:147], v[72:75], a[24:27], v[144:147]\n"
    "ds_read_b128 a[8:11], v31 offset:10240\n"
    "v_mfma_f32_16x16x32_bf16 v[152:155], v[76:79], a[24:27], v[152:155]\n"
    "s_waitcnt lgkmcnt(3)\n"
    "v_mfma_f32_16x16x32_bf16 v[148:151], v[72:75], a[28:31], v[148:151]\n"
    "ds_read_b128 a[12:15], v31 offset:11264\n"
    "v_mfma_f32_16x16x32_bf16 v[156:159], v[76:79], a[28:31], v[156:159]\n"
    "s_waitcnt lgkmcnt(3)\n"
    "v_mfma_f32_16x16x32_bf16 v[160:163], v[208:211], a[0:3], 0\n"
    "ds_read_b128 a[16:19], v31 offset:12288\n"
    "v_mfma_f32_16x16x32_bf16 v[168:171], v[212:215], a[0:3], 0\n"
    "s_waitcnt lgkmcnt(3)\n"
    "v_mfma_f32_16x16x32_bf16 v[164:167], v[208:211], a[4:7], 0\n"
    "ds_read_b128 a[20:23], v31 offset:13312\n"
    "v_mfma_f32_16x16x32_bf16 v[172:175], v[212:215], a[4:7], 0\n"
    "s_waitcnt lgkmcnt(3)\n"
    "v_mfma_f32_16x16x32_bf16 v[160:163], v[240:243], a[8:11], v[160:163]\n"
    "ds_read_b128 a[24:27], v31 offset:14336\n"
    "v_mfma_f32_16x16x32_bf16 v[168:171], v[244:247], a[8:11], v[168:171]\n"
    "s_waitcnt lgkmcnt(3)\n"
    "v_mfma_f32_16x16x32_bf16 v[164:167], v[240:243], a[12:15], v[164:167]\n"
    "ds_read_b128 a[28:31], v31 offset:15360\n"
    "v_cvt_pk_bf16_f32 v88, v144, v145\n"
    "v_mfma_f32_16x16x32_bf16 v[172:175], v[244:247], a[12:15], v[172:175]\n"
    "v_cvt_pk_bf16_f32 v89, v146, v147\n"
    "s_waitcnt lgkmcnt(3)\n"
    "v_mfma_f32_16x16x32_bf16 v[160:163], a[80:83], a[16:19], v[160:163]\n"
    "ds_read_b128 a[0:3], v42 offset:8192\n"
    "v_cvt_pk_bf16_f32 v90, v148, v149\n"
    "v_mfma_f32_16x16x32_bf16 v[168:171], a[84:87], a[16:19], v[168:171]\n"
    "v_cvt_pk_bf16_f32 v91, v150, v151\n"
    "s_waitcnt lgkmcnt(3)\n"
    "v_mfma_f32_16x16x32_bf16 v[164:167], a[80:83], a[20:23], v[164:167]\n"
    "ds_read_b128 a[4:7], v42 offset:9216\n"
    "v_cvt_pk_bf16_f32 v92, v152, v153\n"
    "v_mfma_f32_16x16x32_bf16 v[172:175], a[84:87], a[20:23], v[172:175]\n"
    "v_cvt_pk_bf16_f32 v93, v154, v155\n"
    "s_waitcnt lgkmcnt(3)\n"
    "v_mfma_f32_16x16x32_bf16 v[160:163], a[112:115], a[24:27], v[160:163]\n"
    "ds_read_b128 a[8:11], v42 offset:10240\n"
    "v_cvt_pk_bf16_f32 v94, v156, v157\n"
    "v_mfma_f32_16x16x32_bf16 v[168:171], a[116:119], a[24:27], v[168:171]\n"
    "v_cvt_pk_bf16_f32 v95, v158, v159\n"
    "s_waitcnt lgkmcnt(3)\n"
    "v_mfma_f32_16x16x32_bf16 v[164:167], a[112:115], a[28:31], v[164:167]\n"
    "ds_read_b128 a[12:15], v42 offset:11264\n"
    "buffer_store_dwordx4 v[88:91], v24, s[28:31], 0 offen offset:2048 sc1\n"
    "v_mfma_f32_16x16x32_bf16 v[172:175], a[116:119], a[28:31], v[172:175]\n"
    "buffer_store_dwordx4 v[92:95], v24, s[28:31], 0 offen offset:3072 sc1\n"
    "s_waitcnt lgkmcnt(3)\n"
    "v_mfma_f32_16x16x32_bf16 v[160:163], a[144:147], a[0:3], v[160:163]\n"
    "ds_read_b128 a[16:19], v42 offset:12288\n"
    "v_mfma_f32_16x16x32_bf16 v[168:171], a[148:151], a[0:3], v[168:171]\n"
    "s_waitcnt lgkmcnt(3)\n"
    "v_mfma_f32_16x16x32_bf16 v[164:167], a[144:147], a[4:7], v[164:167]\n"
    "ds_read_b128 a[20:23], v42 offset:13312\n"
    "v_mfma_f32_16x16x32_bf16 v[172:175], a[148:151], a[4:7], v[172:175]\n"
    "s_waitcnt lgkmcnt(3)\n"
    "v_mfma_f32_16x16x32_bf16 v[160:163], a[176:179], a[8:11], v[160:163]\n"
    "ds_read_b128 a[24:27], v42 offset:14336\n"
    "v_mfma_f32_16x16x32_bf16 v[168:171], a[180:183], a[8:11], v[168:171]\n"
    "s_waitcnt lgkmcnt(3)\n"
    "v_mfma_f32_16x16x32_bf16 v[164:167], a[176:179], a[12:15], v[164:167]\n"
    "ds_read_b128 a[28:31], v42 offset:15360\n"
    "v_mfma_f32_16x16x32_bf16 v[172:175], a[180:183], a[12:15], v[172:175]\n"
    "s_waitcnt lgkmcnt(3)\n"
    "v_mfma_f32_16x16x32_bf16 v[160:163], a[208:211], a[16:19], v[160:163]\n"
    "ds_read_b128 a[0:3], v31 offset:8192\n"
    "v_mfma_f32_16x16x32_bf16 v[168:171], a[212:215], a[16:19], v[168:171]\n"
    "s_waitcnt lgkmcnt(3)\n"
    "v_mfma_f32_16x16x32_bf16 v[164:167], a[208:211], a[20:23], v[164:167]\n"
    "ds_read_b128 a[4:7], v31 offset:9216\n"
    "v_mfma_f32_16x16x32_bf16 v[172:175], a[212:215], a[20:23], v[172:175]\n"
    "s_waitcnt lgkmcnt(3)\n"
    "v_mfma_f32_16x16x32_bf16 v[160:163], v[96:99], a[24:27], v[160:163]\n"
    "ds_read_b128 a[8:11], v31 offset:10240\n"
    "v_mfma_f32_16x16x32_bf16 v[168:171], v[100:103], a[24:27], v[168:171]\n"
    "s_waitcnt lgkmcnt(3)\n"
    "v_mfma_f32_16x16x32_bf16 v[164:167], v[96:99], a[28:31], v[164:167]\n"
    "ds_read_b128 a[12:15], v31 offset:11264\n"
    "v_mfma_f32_16x16x32_bf16 v[172:175], v[100:103], a[28:31], v[172:175]\n"
    "s_waitcnt lgkmcnt(3)\n"
    "v_mfma_f32_16x16x32_bf16 v[176:179], v[216:219], a[0:3], 0\n"
    "ds_read_b128 a[16:19], v31 offset:12288\n"
    "v_mfma_f32_16x16x32_bf16 v[184:187], v[220:223], a[0:3], 0\n"
    "s_waitcnt lgkmcnt(3)\n"
    "v_mfma_f32_16x16x32_bf16 v[180:183], v[216:219], a[4:7], 0\n"
    "ds_read_b128 a[20:23], v31 offset:13312\n"
    "v_mfma_f32_16x16x32_bf16 v[188:191], v[220:223], a[4:7], 0\n"
    "s_waitcnt lgkmcnt(3)\n"
    "v_mfma_f32_16x16x32_bf16 v[176:179], v[248:251], a[8:11], v[176:179]\n"
    "ds_read_b128 a[24:27], v31 offset:14336\n"
    "v_mfma_f32_16x16x32_bf16 v[184:187], v[252:255], a[8:11], v[184:187]\n"
    "s_waitcnt lgkmcnt(3)\n"
    "v_mfma_f32_16x16x32_bf16 v[180:183], v[248:251], a[12:15], v[180:183]\n"
    "ds_read_b128 a[28:31], v31 offset:15360\n"
    "v_cvt_pk_bf16_f32 v112, v160, v161\n"
    "v_mfma_f32_16x16x32_bf16 v[188:191], v[252:255], a[12:15], v[188:191]\n"
    "v_cvt_pk_bf16_f32 v113, v162, v163\n"
    "s_waitcnt lgkmcnt(3)\n"
    "v_mfma_f32_16x16x32_bf16 v[176:179], a[88:91], a[16:19], v[176:179]\n"
    "ds_read_b128 a[0:3], v42 offset:8192\n"
    "v_cvt_pk_bf16_f32 v114, v164, v165\n"
    "v_mfma_f32_16x16x32_bf16 v[184:187], a[92:95], a[16:19], v[184:187]\n"
    "v_cvt_pk_bf16_f32 v115, v166, v167\n"
    "s_waitcnt lgkmcnt(3)\n"
    "v_mfma_f32_16x16x32_bf16 v[180:183], a[88:91], a[20:23], v[180:183]\n"
    "ds_read_b128 a[4:7], v42 offset:9216\n"
    "v_cvt_pk_bf16_f32 v116, v168, v169\n"
    "v_mfma_f32_16x16x32_bf16 v[188:191], a[92:95], a[20:23], v[188:191]\n"
    "v_cvt_pk_bf16_f32 v117, v170, v171\n"
    "s_waitcnt lgkmcnt(3)\n"
    "v_mfma_f32_16x16x32_bf16 v[176:179], a[120:123], a[24:27], v[176:179]\n"
    "ds_read_b128 a[8:11], v42 offset:10240\n"
    "v_cvt_pk_bf16_f32 v118, v172, v173\n"
    "v_mfma_f32_16x16x32_bf16 v[184:187], a[124:127], a[24:27], v[184:187]\n"
    "v_cvt_pk_bf16_f32 v119, v174, v175\n"
    "s_waitcnt lgkmcnt(3)\n"
    "v_mfma_f32_16x16x32_bf16 v[180:183], a[120:123], a[28:31], v[180:183]\n"
    "ds_read_b128 a[12:15], v42 offset:11264\n"
    "s_mov_b32 s45, 4096\n"
    "v_mfma_f32_16x16x32_bf16 v[188:191], a[124:127], a[28:31], v[188:191]\n"
    "buffer_store_dwordx4 v[112:115], v24, s[28:31], s45 offen sc1\n"
    "s_waitcnt lgkmcnt(3)\n"
    "v_mfma_f32_16x16x32_bf16 v[176:179], a[152:155], a[0:3], v[176:179]\n"
    "ds_read_b128 a[16:19], v42 offset:12288\n"
    "buffer_store_dwordx4 v[116:119], v24, s[28:31], s45 offen offset:1024 sc1\n"
    "v_mfma_f32_16x16x32_bf16 v[184:187], a[156:159], a[0:3], v[184:187]\n"
    "s_waitcnt lgkmcnt(3)\n"
    "v_mfma_f32_16x16x32_bf16 v[180:183], a[152:155], a[4:7], v[180:183]\n"
    "ds_read_b128 a[20:23], v42 offset:13312\n"
    "v_mfma_f32_16x16x32_bf16 v[188:191], a[156:159], a[4:7], v[188:191]\n"
    "s_waitcnt lgkmcnt(3)\n"
    "v_mfma_f32_16x16x32_bf16 v[176:179], a[184:187], a[8:11], v[176:179]\n"
    "ds_read_b128 a[24:27], v42 offset:14336\n"
    "v_mfma_f32_16x16x32_bf16 v[184:187], a[188:191], a[8:11], v[184:187]\n"
    "s_waitcnt lgkmcnt(3)\n"
    "v_mfma_f32_16x16x32_bf16 v[180:183], a[184:187], a[12:15], v[180:183]\n"
    "ds_read_b128 a[28:31], v42 offset:15360\n"
    "v_mfma_f32_16x16x32_bf16 v[188:191], a[188:191], a[12:15], v[188:191]\n"
    "s_waitcnt lgkmcnt(3)\n"
    "v_mfma_f32_16x16x32_bf16 v[176:179], a[216:219], a[16:19], v[176:179]\n"
    "v_mfma_f32_16x16x32_bf16 v[184:187], a[220:223], a[16:19], v[184:187]\n"
    "s_waitcnt lgkmcnt(2)\n"
    "v_mfma_f32_16x16x32_bf16 v[180:183], a[216:219], a[20:23], v[180:183]\n"
    "v_mfma_f32_16x16x32_bf16 v[188:191], a[220:223], a[20:23], v[188:191]\n"
    "s_waitcnt lgkmcnt(1)\n"
    "v_mfma_f32_16x16x32_bf16 v[176:179], v[104:107], a[24:27], v[176:179]\n"
    "v_mfma_f32_16x16x32_bf16 v[184:187], v[108:111], a[24:27], v[184:187]\n"
    "s_waitcnt lgkmcnt(0)\n"
    "v_mfma_f32_16x16x32_bf16 v[180:183], v[104:107], a[28:31], v[180:183]\n"
    "v_mfma_f32_16x16x32_bf16 v[188:191], v[108:111], a[28:31], v[188:191]\n"
    "s_waitcnt vmcnt(0)\n"
    "s_mov_b64 exec, 1\n"
    "buffer_store_dwordx2 v[34:35], v26, s[28:31], 0 offen sc1\n"
    "s_mov_b64 exec, -1\n"
    "buffer_load_dwordx2 v[36:37], v27, s[28:31], 0 offen sc1\n"
    "s_mov_b32 s44, 0x40000\n"
    "Lr_spin_%=:\n"
    "s_sleep 3\n"
    "buffer_load_dwordx2 v[62:63], v27, s[28:31], 0 offen sc1\n"
    "s_waitcnt vmcnt(1)\n"
    "v_cmp_ne_u64_e64 s[50:51], v[36:37], v[34:35]\n"
    "s_nop 4\n"
    "s_cmp_eq_u64 s[50:51], 0\n"
    "s_cbranch_scc1 Lr_ok_%=\n"
    "s_sleep 3\n"
    "buffer_load_dwordx2 v[36:37], v27, s[28:31], 0 offen sc1\n"
    "s_waitcnt vmcnt(1)\n"
    "v_cmp_ne_u64_e64 s[50:51], v[62:63], v[34:35]\n"
    "s_nop 4\n"
    "s_cmp_eq_u64 s[50:51], 0\n"
    "s_cbranch_scc1 Lr_ok_%=\n"
    "s_sub_u32 s44, s44, 1\n"
    "s_cmp_lg_u32 s44, 0\n"
    "s_cbranch_scc1 Lr_spin_%=\n"
    "Lr_ok_%=:\n"
    "buffer_load_dwordx4 v[192:195], v38, s[28:31], 0 offen sc1\n"
    "buffer_load_dwordx4 v[196:199], v38, s[28:31], 0 offen offset:1024 sc1\n"
    "buffer_load_dwordx4 v[200:203], v39, s[28:31], 0 offen sc1\n"
    "buffer_load_dwordx4 v[204:207], v39, s[28:31], 0 offen offset:1024 sc1\n"
    "buffer_load_dwordx4 v[208:211], v40, s[28:31], 0 offen sc1\n"
    "buffer_load_dwordx4 v[212:215], v40, s[28:31], 0 offen offset:1024 sc1\n"
    "s_mov_b64 exec, s[42:43]\n"
    "buffer_store_dwordx2 v[34:35], v30, s[28:31], 0 offen sc1\n"
    "s_mov_b64 exec, -1\n"
    "s_waitcnt vmcnt(5)\n"
    "v_lshlrev_b32 v56, 16, v192\n"
    "v_and_b32 v57, 0xffff0000, v192\n"
    "v_add_f32 v176, v176, v56\n"
    "v_add_f32 v177, v177, v57\n"
    "v_lshlrev_b32 v56, 16, v193\n"
    "v_and_b32 v57, 0xffff0000, v193\n"
    "v_add_f32 v178, v178, v56\n"
    "v_add_f32 v179, v179, v57\n"
    "v_lshlrev_b32 v56, 16, v194\n"
    "v_and_b32 v57, 0xffff0000, v194\n"
    "v_add_f32 v180, v180, v56\n"
    "v_add_f32 v181, v181, v57\n"
    "v_lshlrev_b32 v56, 16, v195\n"
    "v_and_b32 v57, 0xffff0000, v195\n"
    "v_add_f32 v182, v182, v56\n"
    "v_add_f32 v183, v183, v57\n"
    "s_waitcnt vmcnt(4)\n"
    "v_lshlrev_b32 v56, 16, v196\n"
    "v_and_b32 v57, 0xffff0000, v196\n"
    "v_add_f32 v184, v184, v56\n"
    "v_add_f32 v185, v185, v57\n"
    "v_lshlrev_b32 v56, 16, v197\n"
    "v_and_b32 v57, 0xffff0000, v197\n"
    "v_add_f32 v186, v186, v56\n"
    "v_add_f32 v187, v187, v57\n"
    "v_lshlrev_b32 v56, 16, v198\n"
    "v_and_b32 v57, 0xffff0000, v198\n"
    "v_add_f32 v188, v188, v56\n"
    "v_add_f32 v189, v189, v57\n"
    "v_lshlrev_b32 v56, 16, v199\n"
    "v_and_b32 v57, 0xffff0000, v199\n"
    "v_add_f32 v190, v190, v56\n"
    "v_add_f32 v191, v191, v57\n"
    "s_waitcnt vmcnt(3)\n"
    "v_lshlrev_b32 v56, 16, v200\n"
    "v_and_b32 v57, 0xffff0000, v200\n"
    "v_add_f32 v176, v176, v56\n"
    "v_add_f32 v177, v177, v57\n"
    "v_lshlrev_b32 v56, 16, v201\n"
    "v_and_b32 v57, 0xffff0000, v201\n"
    "v_add_f32 v178, v178, v56\n"
    "v_add_f32 v179, v179, v57\n"
    "v_lshlrev_b32 v56, 16, v202\n"
    "v_and_b32 v57, 0xffff0000, v202\n"
    "v_add_f32 v180, v180, v56\n"
    "v_add_f32 v181, v181, v57\n"
    "v_lshlrev_b32 v56, 16, v203\n"
    "v_and_b32 v57, 0xffff0000, v203\n"
    "v_add_f32 v182, v182, v56\n"
    "v_add_f32 v183, v183, v57\n"
    "s_waitcnt vmcnt(2)\n"
    "v_lshlrev_b32 v56, 16, v204\n"
    "v_and_b32 v57, 0xffff0000, v204\n"
    "v_add_f32 v184, v184, v56\n"
    "v_add_f32 v185, v185, v57\n"
    "v_lshlrev_b32 v56, 16, v205\n"
    "v_and_b32 v57, 0xffff0000, v205\n"
    "v_add_f32 v186, v186, v56\n"
    "v_add_f32 v187, v187, v57\n"
    "v_lshlrev_b32 v56, 16, v206\n"
    "v_and_b32 v57, 0xffff0000, v206\n"
    "v_add_f32 v188, v188, v56\n"
    "v_add_f32 v189, v189, v57\n"
    "v_lshlrev_b32 v56, 16, v207\n"
    "v_and_b32 v57, 0xffff0000, v207\n"
    "v_add_f32 v190, v190, v56\n"
    "v_add_f32 v191, v191, v57\n"
    "s_waitcnt vmcnt(1)\n"
    "v_lshlrev_b32 v56, 16, v208\n"
    "v_and_b32 v57, 0xffff0000, v208\n"
    "v_add_f32 v176, v176, v56\n"
    "v_add_f32 v177, v177, v57\n"
    "v_lshlrev_b32 v56, 16, v209\n"
    "v_and_b32 v57, 0xffff0000, v209\n"
    "v_add_f32 v178, v178, v56\n"
    "v_add_f32 v179, v179, v57\n"
    "v_lshlrev_b32 v56, 16, v210\n"
    "v_and_b32 v57, 0xffff0000, v210\n"
    "v_add_f32 v180, v180, v56\n"
    "v_add_f32 v181, v181, v57\n"
    "v_lshlrev_b32 v56, 16, v211\n"
    "v_and_b32 v57, 0xffff0000, v211\n"
    "v_add_f32 v182, v182, v56\n"
    "v_add_f32 v183, v183, v57\n"
    "s_waitcnt vmcnt(0)\n"
    "v_lshlrev_b32 v56, 16, v212\n"
    "v_and_b32 v57, 0xffff0000, v212\n"
    "v_add_f32 v184, v184, v56\n"
    "v_add_f32 v185, v185, v57\n"
    "v_lshlrev_b32 v56, 16, v213\n"
    "v_and_b32 v57, 0xffff0000, v213\n"
    "v_add_f32 v186, v186, v56\n"
    "v_add_f32 v187, v187, v57\n"
    "v_lshlrev_b32 v56, 16, v214\n"
    "v_and_b32 v57, 0xffff0000, v214\n"
    "v_add_f32 v188, v188, v56\n"
    "v_add_f32 v189, v189, v57\n"
    "v_lshlrev_b32 v56, 16, v215\n"
    "v_and_b32 v57, 0xffff0000, v215\n"
    "v_add_f32 v190, v190, v56\n"
    "v_add_f32 v191, v191, v57\n"
    "v_cvt_pk_bf16_f32 v232, v176, v177\n"
    "v_cvt_pk_bf16_f32 v233, v178, v179\n"
    "v_cvt_pk_bf16_f32 v234, v180, v181\n"
    "v_cvt_pk_bf16_f32 v235, v182, v183\n"
    "v_cvt_pk_bf16_f32 v236, v184, v185\n"
    "v_cvt_pk_bf16_f32 v237, v186, v187\n"
    "v_cvt_pk_bf16_f32 v238, v188, v189\n"
    "v_cvt_pk_bf16_f32 v239, v190, v191\n"
    "buffer_store_dwordx2 v[232:233], v28, s[32:35], 0 offen\n"
    "buffer_store_dwordx2 v[234:235], v28, s[32:35], s46 offen\n"
    "buffer_store_dwordx2 v[236:237], v28, s[32:35], 0 offen offset:32\n"
    "buffer_store_dwordx2 v[238:239], v28, s[32:35], s46 offen offset:32\n"
    "s_waitcnt vmcnt(0)\n"
    :
    : "{s[8:11]}"(sx), "{s[12:15]}"(swq), "{s[16:19]}"(sk), "{s[20:23]}"(sv), "{s[24:27]}"(swo), "{s[28:31]}"(sws), "{s[32:35]}"(sout),
      "{s36}"(sM0), "{s37}"(sM0P), "{s39}"(scale), "{s[40:41]}"(bumpP), "{s[42:43]}"(bumpR), "{s47}"(rfl(r)), "{s[56:57]}"(perm1), "{s58}"(perm2), "{s61}"(cls),
      "{v[4:7]}"(aRT), "{v[8:11]}"(aTm), "{v[12:15]}"(voV), "{v[16:19]}"(voWo), "{v[20:23]}"(vP4), "{v[24:27]}"(vR4), "{v[28:31]}"(vO4),
      "{v[38:41]}"(vTmp), "{v[192:195]}"(aR1), "{v[204:207]}"(aK), "{v[216:219]}"(voX), "{v[220:223]}"(voWq), "{v[224:227]}"(voK)
    : "v0", "v1", "v2", "v3", "v32", "v33", "v34", "v35", "v36", "v37", "v42", "v43", "v44", "v45", "v46", "v47", "v48", "v49", "v50", "v51", "v52", "v53", "v54", "v55", "v56", "v57", "v58", "v59", "v60", "v61", "v62", "v63", "v64", "v65", "v66", "v67", "v68", "v69", "v70", "v71", "v72", "v73", "v74", "v75", "v76", "v77", "v78", "v79", "v80", "v81", "v82", "v83", "v84", "v85", "v86", "v87", "v88", "v89", "v90", "v91", "v92", "v93", "v94", "v95", "v96", "v97", "v98", "v99", "v100", "v101", "v102", "v103", "v104", "v105", "v106", "v107", "v108", "v109", "v110", "v111", "v112", "v113", "v114", "v115", "v116", "v117", "v118", "v119", "v120", "v121", "v122", "v123", "v124", "v125", "v126", "v127", "v128", "v129", "v130", "v131", "v132", "v133", "v134", "v135", "v136", "v137", "v138", "v139", "v140", "v141", "v142", "v143", "v144", "v145", "v146", "v147", "v148", "v149", "v150", "v151", "v152", "v153", "v154", "v155", "v156", "v157", "v158", "v159", "v160", "v161", "v162", "v163", "v164", "v165", "v166", "v167", "v168", "v169", "v170", "v171", "v172", "v173", "v174", "v175", "v176", "v177", "v178", "v179", "v180", "v181", "v182", "v183", "v184", "v185", "v186", "v187", "v188", "v189", "v190", "v191", "v196", "v197", "v198", "v199", "v200", "v201", "v202", "v203", "v208", "v209", "v210", "v211", "v212", "v213", "v214", "v215", "v228", "v229", "v230", "v231", "v232", "v233", "v234", "v235", "v236", "v237", "v238", "v239", "v240", "v241", "v242", "v243", "v244", "v245", "v246", "v247", "v248", "v249", "v250", "v251", "v252", "v253", "v254", "v255", "a0", "a1", "a2", "a3", "a4", "a5", "a6", "a7", "a8", "a9", "a10", "a11", "a12", "a13", "a14", "a15", "a16", "a17", "a18", "a19", "a20", "a21", "a22", "a23", "a24", "a25", "a26", "a27", "a28", "a29", "a30", "a31", "a32", "a33", "a34", "a35", "a36", "a37", "a38", "a39", "a40", "a41", "a42", "a43", "a44", "a45", "a46", "a47", "a48", "a49", "a50", "a51", "a52", "a53", "a54", "a55", "a56", "a57", "a58", "a59", "a60", "a61", "a62", "a63", "a64", "a65", "a66", "a67", "a68", "a69", "a70", "a71", "a72", "a73", "a74", "a75", "a76", "a77", "a78", "a79", "a80", "a81", "a82", "a83", "a84", "a85", "a86", "a87", "a88", "a89", "a90", "a91", "a92", "a93", "a94", "a95", "a96", "a97", "a98", "a99", "a100", "a101", "a102", "a103", "a104", "a105", "a106", "a107", "a108", "a109", "a110", "a111", "a112", "a113", "a114", "a115", "a116", "a117", "a118", "a119", "a120", "a121", "a122", "a123", "a124", "a125", "a126", "a127", "a128", "a129", "a130", "a131", "a132", "a133", "a134", "a135", "a136", "a137", "a138", "a139", "a140", "a141", "a142", "a143", "a144", "a145", "a146", "a147", "a148", "a149", "a150", "a151", "a152", "a153", "a154", "a155", "a156", "a157", "a158", "a159", "a160", "a161", "a162", "a163", "a164", "a165", "a166", "a167", "a168", "a169", "a170", "a171", "a172", "a173", "a174", "a175", "a176", "a177", "a178", "a179", "a180", "a181", "a182", "a183", "a184", "a185", "a186", "a187", "a188", "a189", "a190", "a191", "a192", "a193", "a194", "a195", "a196", "a197", "a198", "a199", "a200", "a201", "a202", "a203", "a204", "a205", "a206", "a207", "a208", "a209", "a210", "a211", "a212", "a213", "a214", "a215", "a216", "a217", "a218", "a219", "a220", "a221", "a222", "a223", "a224", "a225", "a226", "a227", "a228", "a229", "a230", "a231", "a232", "a233", "a234", "a235", "a236", "a237", "a238", "a239", "a240", "a241", "a242", "a243", "a244", "a245", "a246", "a247", "a248", "a249", "a250", "a251", "a252", "a253", "a254", "a255", "s38", "s44", "s45", "s46", "s48", "s49", "s50", "s51", "s52", "s53", "s54", "s55", "s59", "s60", "s62", "s63", "s64", "s65", "s66", "s67", "s68", "s69", "s70", "s71", "s72", "s73", "s74", "s75", "s76", "s77", "s78", "s79", "s80", "s81", "s82", "s83", "s84", "s85", "s86", "s87", "s88", "s89", "s90", "s91", "s92", "s93", "s94", "s95", "s96", "s97", "s98", "s99", "vcc", "scc", "memory");
}

extern "C" void kernel_launch(void* const* d_in, const int* in_sizes, int n_in, void* d_out, int out_size, void* d_ws, size_t ws_size, hipStream_t stream) {
  (void)in_sizes; (void)n_in; (void)out_size; (void)ws_size;
  (void)hipFuncSetAttribute((const void*)fused_attn_pair, hipFuncAttributeMaxDynamicSharedMemorySize, LDS_BYTES);
  fused_attn_pair<<<256, 256, LDS_BYTES, stream>>>(d_in[0], d_in[1], d_in[2], d_in[3], d_in[4], d_in[5], d_in[6], d_in[7], d_in[8], d_in[9], d_out, d_ws);
}
```
